```python
import torch

def _softmax(x, axis=-1):
    return torch.softmax(x, dim=axis)
def _gelu(x):
    return torch.nn.functional.gelu(x, approximate="tanh")
def _silu(x):
    return torch.nn.functional.silu(x)
def _sigmoid(x):
    return torch.sigmoid(x)
def _layernorm(x, axis=-1, eps=1e-5):
    m = x.mean(dim=axis, keepdim=True)
    v = x.var(dim=axis, keepdim=True, unbiased=False)
    return (x - m) / torch.sqrt(v + eps)
def _scatter_add(tgt, idx, src, axis=0):
    return tgt.clone().index_add_(axis, idx, src)
def _segment_sum(src, seg, n):
    out = torch.zeros((n,) + src.shape[1:], dtype=src.dtype)
    return out.index_add_(0, seg, src)
def _pad(x, pw):
    flat = tuple(v for pair in reversed(pw) for v in pair)
    return torch.nn.functional.pad(x, flat)

kernel_name = 'gated_mix__ssm_conv_h3__glu_gated_swiglu__prompt'

def setup_inputs():

    _w = (lambda a: (lambda t: t.to(torch.bfloat16) if t.is_floating_point() else t)(torch.as_tensor(a)))
    def _A_setup():
        B, S, D = 1, 2048, 576
        ks = 4
        return {
            "q": _w((lambda *s: torch.randn(*s, dtype=torch.bfloat16) * 0.05)(B, S, D)),
            "k": _w((lambda *s: torch.randn(*s, dtype=torch.bfloat16) * 0.05)(B, S, D)),
            "v": _w((lambda *s: torch.randn(*s, dtype=torch.bfloat16) * 0.05)(B, S, D)),
            "shift_k": _w((lambda *s: torch.randn(*s, dtype=torch.bfloat16) * 0.05)(D, ks) * 0.5),
            "ssm_k": _w((lambda *s: torch.randn(*s, dtype=torch.bfloat16) * 0.05)(D, S) * (1.0 / S ** 0.5)),
            "ssm_D": _w((lambda *s: torch.randn(*s, dtype=torch.bfloat16) * 0.05)(D)),
        }
    _A_in = _A_setup()

    def _B_setup():
        B, S, D, F = 1, 2048, 576, 4608
        return {
            "x": _w((lambda *s: torch.randn(*s, dtype=torch.bfloat16) * 0.05)(B, S, D)),
            "w1": _w((lambda *s: torch.randn(*s, dtype=torch.bfloat16) * 0.05)(D, F) * (2.0 / D) ** 0.5),
            "w3": _w((lambda *s: torch.randn(*s, dtype=torch.bfloat16) * 0.05)(D, F) * (2.0 / D) ** 0.5),
            "w2": _w((lambda *s: torch.randn(*s, dtype=torch.bfloat16) * 0.05)(F, D) * (2.0 / F) ** 0.5),
        }
    _B_in = _B_setup()

    return {
        "A_q": _A_in["q"],
        "A_k": _A_in["k"],
        "A_v": _A_in["v"],
        "A_shift_k": _A_in["shift_k"],
        "A_ssm_k": _A_in["ssm_k"],
        "A_ssm_D": _A_in["ssm_D"],
        "B_x": _B_in["x"],
        "B_w1": _B_in["w1"],
        "B_w3": _B_in["w3"],
        "B_w2": _B_in["w2"],
        "gate_w": _w((lambda *s: torch.randn(*s, dtype=torch.bfloat16) * 0.05)(576) * 16.0),
    }

def reference(A_q, A_k, A_v, A_shift_k, A_ssm_k, A_ssm_D, B_x, B_w1, B_w3, B_w2, gate_w):
    def _A_ref(q, k, v, shift_k, ssm_k, ssm_D):
        B, S, D = q.shape
        ks = shift_k.shape[-1]
        k_pad = _pad(k, ((0, 0), (ks - 1, 0), (0, 0)))
        k_shift = torch.zeros_like(k)
        for t in range(ks):
            k_shift = k_shift + k_pad[:, t:t + S, :] * shift_k[:, ks - 1 - t][None, None, :]
        kv = k_shift * v
        L = 2 * S
        kv_pad = _pad(kv, ((0, 0), (0, S), (0, 0)))
        kf_pad = _pad(ssm_k, ((0, 0), (0, S)))
        kv_f = (lambda x, axes=(-2,-1): torch.fft.rfft2(x.float(), dim=axes))(kv_pad[:, None, :, :], axes=(1, 2))
        kf_f = (lambda x, axes=(-2,-1): torch.fft.rfft2(x.float(), dim=axes))(kf_pad[:, None, :], axes=(1, 2))
        kf_f = (lambda x, *d: x.permute(*d))(kf_f, 1, 2, 0)
        y_f = kv_f * kf_f[None, :, :, :]
        y = (lambda x, s, axes=(-2,-1): torch.fft.irfft2(x, s=s, dim=axes).to(torch.bfloat16))(y_f, (1, L), axes=(1, 2))[:, 0, :S, :]
        y = y + kv * ssm_D[None, None, :]
        return q * y

    def _B_ref(x, w1, w3, w2):
        gate = torch.einsum("bsd,df->bsf", x, w1)
        up = torch.einsum("bsd,df->bsf", x, w3)
        h = _silu(gate) * up
        out = torch.einsum("bsf,fd->bsd", h, w2)
        return out

    x = A_q
    g = _sigmoid((x * gate_w).sum(-1, keepdims=True))
    a = _A_ref(x, A_k, A_v, A_shift_k, A_ssm_k, A_ssm_D)
    b = _B_ref(x, B_w1, B_w3, B_w2)
    return g * a + (1 - g) * b
```

```cpp
#include <hip/hip_runtime.h>
#include <hip/hip_bf16.h>
#include <stdint.h>

typedef unsigned short u16;
constexpr int S = 2048, D = 576, F = 4608;

__device__ __forceinline__ float bf2f(u16 b) { return __uint_as_float(((unsigned)b) << 16); }
__device__ __forceinline__ u16 f2bf(float f) { __hip_bfloat16 h = __float2bfloat16(f); return __builtin_bit_cast(u16, h); }
__device__ __forceinline__ float rbf(float f) { return bf2f(f2bf(f)); }

__global__ void k_kv(const u16* q, const u16* k, const u16* v, const u16* sk, const u16* gw, u16* kv, float* g) {
    int t = blockIdx.x;
    __shared__ float red[4];
    float acc = 0.f;
    for (int d = threadIdx.x; d < D; d += 256) {
        float ks = 0.f;
        for (int j = 0; j < 4; ++j) {
            int tt = t + j - 3;
            float kk = tt >= 0 ? bf2f(k[tt * D + d]) : 0.f;
            ks = rbf(ks + rbf(kk * bf2f(sk[d * 4 + 3 - j])));
        }
        kv[t * D + d] = f2bf(ks * bf2f(v[t * D + d]));
        acc += rbf(bf2f(q[t * D + d]) * bf2f(gw[d]));
    }
    for (int o = 32; o > 0; o >>= 1) acc += __shfl_down(acc, o);
    if ((threadIdx.x & 63) == 0) red[threadIdx.x >> 6] = acc;
    __syncthreads();
    if (threadIdx.x == 0) {
        float s = rbf(red[0] + red[1] + red[2] + red[3]);
        g[t] = rbf(1.f / (1.f + expf(-s)));
    }
}

__global__ void k_conv(const u16* kv, const u16* hk, float* y) {
    int d = blockIdx.x;
    __shared__ float u[S], h[S];
    for (int t = threadIdx.x; t < S; t += 256) { u[t] = bf2f(kv[t * D + d]); h[t] = bf2f(hk[d * S + t]); }
    __syncthreads();
    for (int t = threadIdx.x; t < S; t += 256) {
        float acc = 0.f;
        for (int s = 0; s <= t; ++s) acc += h[t - s] * u[s];
        y[t * D + d] = acc;
    }
}

__global__ void k_gemm1(const u16* x, const u16* w1, const u16* w3, u16* h) {
    int f = blockIdx.x * 256 + threadIdx.x;
    int t0 = blockIdx.y * 8;
    __shared__ float xs[8][D];
    for (int i = threadIdx.x; i < 8 * D; i += 256) xs[i / D][i % D] = bf2f(x[t0 * D + i]);
    __syncthreads();
    float g[8] = {}, u[8] = {};
    for (int d = 0; d < D; ++d) {
        float a = bf2f(w1[d * F + f]), b = bf2f(w3[d * F + f]);
        for (int i = 0; i < 8; ++i) { g[i] += xs[i][d] * a; u[i] += xs[i][d] * b; }
    }
    for (int i = 0; i < 8; ++i) {
        float gg = rbf(g[i]), uu = rbf(u[i]);
        float s = rbf(gg / (1.f + expf(-gg)));
        h[(t0 + i) * F + f] = f2bf(s * uu);
    }
}

__global__ void k_gemm2(const u16* h, const u16* w2, const u16* q, const u16* kv, const float* y, const u16* sD, const float* g, u16* out) {
    int n = threadIdx.x;
    int t0 = blockIdx.x * 4;
    __shared__ float hs[4][F];
    for (int i = threadIdx.x; i < 4 * F; i += 576) hs[i / F][i % F] = bf2f(h[t0 * F + i]);
    __syncthreads();
    float acc[4] = {};
    for (int f = 0; f < F; ++f) {
        float w = bf2f(w2[f * D + n]);
        for (int i = 0; i < 4; ++i) acc[i] += hs[i][f] * w;
    }
    for (int i = 0; i < 4; ++i) {
        int t = t0 + i;
        float b = rbf(acc[i]);
        float yy = rbf(y[t * D + n]);
        float kvv = bf2f(kv[t * D + n]);
        yy = rbf(yy + rbf(kvv * bf2f(sD[n])));
        float a = rbf(bf2f(q[t * D + n]) * yy);
        float gg = g[t];
        float o = rbf(gg * a) + rbf(rbf(1.f - gg) * b);
        out[t * D + n] = f2bf(o);
    }
}

extern "C" void kernel_launch(void* const* d_in, const int* in_sizes, int n_in,
                              void* d_out, int out_size, void* d_ws, size_t ws_size,
                              hipStream_t stream) {
    const u16* q = (const u16*)d_in[0];
    const u16* k = (const u16*)d_in[1];
    const u16* v = (const u16*)d_in[2];
    const u16* sk = (const u16*)d_in[3];
    const u16* hk = (const u16*)d_in[4];
    const u16* sD = (const u16*)d_in[5];
    const u16* w1 = (const u16*)d_in[7];
    const u16* w3 = (const u16*)d_in[8];
    const u16* w2 = (const u16*)d_in[9];
    const u16* gw = (const u16*)d_in[10];
    char* ws = (char*)d_ws;
    u16* kv = (u16*)ws;
    float* g = (float*)(ws + (4 << 20));
    float* y = (float*)(ws + (8 << 20));
    u16* h = (u16*)(ws + (16 << 20));
    k_kv<<<S, 256, 0, stream>>>(q, k, v, sk, gw, kv, g);
    k_conv<<<D, 256, 0, stream>>>(kv, hk, y);
    k_gemm1<<<dim3(F / 256, S / 8), 256, 0, stream>>>(q, w1, w3, h);
    k_gemm2<<<S / 4, 576, 0, stream>>>(h, w2, q, kv, y, sD, g, (u16*)d_out);
}
```

```cpp
#include <hip/hip_runtime.h>
#include <hip/hip_bf16.h>
#include <stdint.h>
#define PA 2
#define PB 16
#define CLK "s_memrealtime"
#define DIV 1
#define DBG_PARAM
#define DBG_STAMP(i) do { if ((i) == PA || (i) == PB) { __builtin_amdgcn_sched_barrier(0); unsigned long long t_; asm volatile(CLK " %0\n\ts_waitcnt lgkmcnt(0)" : "=s"(t_) :: "memory"); if ((i) == PA) tA = t_; else tB = t_; __builtin_amdgcn_sched_barrier(0); } \
    if ((i) == 7) { unsigned long long e_ = __builtin_amdgcn_s_memrealtime() + (tB - tA) / DIV; while (__builtin_amdgcn_s_memrealtime() < e_) __builtin_amdgcn_s_sleep(1); } } while (0)
#define DBG_STAMPT(i, t)
typedef unsigned short u16;
typedef __attribute__((ext_vector_type(8))) short bf16x8;
typedef __attribute__((ext_vector_type(4))) short s16x4;
typedef __attribute__((ext_vector_type(4))) float f32x4;
typedef __attribute__((ext_vector_type(2))) float f32x2;
typedef __attribute__((ext_vector_type(16))) float f32x16;
typedef __attribute__((ext_vector_type(4))) unsigned u32x4;
typedef __attribute__((ext_vector_type(2))) unsigned u32x2;
typedef short v4i16_t __attribute__((ext_vector_type(4)));
#define LDSP __attribute__((address_space(3)))
#define DI __device__ __forceinline__
#define SBAR() __builtin_amdgcn_sched_barrier(0)
#define LAUNDER(x) asm volatile("" : "+v"(x))
#define LAUNDER_S(x) asm volatile("" : "+s"(x))

constexpr int S_ = 2048, D_ = 576, F_ = 4608;

DI float asf(unsigned u) { return __builtin_bit_cast(float, u); }
DI unsigned pk2bf(float lo, float hi) {
    typedef __attribute__((ext_vector_type(2))) __bf16 bf2;
    typedef __attribute__((ext_vector_type(2))) float f2;
    f2 v = {lo, hi};
    bf2 r = __builtin_convertvector(v, bf2);
    return __builtin_bit_cast(unsigned, r);
}
DI float rbf(float f) { return asf(pk2bf(0.f, f)); }
DI float bflo(unsigned w) { return asf(w << 16); }
DI float bfhi(unsigned w) { return asf(w & 0xffff0000u); }
DI bool wave_all(bool p) { return __builtin_amdgcn_ballot_w64(p) == ~0ull; }

#define NT_IN ""
#define NT_KV " nt"
#define NT_T ""
#define NT_W2 ""
#define NT_ST ""
#define GLDS16(voff, sbase, ldsaddr, SC) asm volatile("s_mov_b32 m0, %2\n\ts_nop 0\n\tglobal_load_lds_dwordx4 %0, %1" SC :: "v"(voff), "s"(sbase), "s"(ldsaddr) : "memory")
DI void glds_full(unsigned voff, const void* sbase, unsigned ldsaddr) { GLDS16(voff, sbase, ldsaddr, NT_IN); }
DI void glds_sc1(unsigned voff, const void* sbase, unsigned ldsaddr) { GLDS16(voff, sbase, ldsaddr, " sc1"); }
DI void glds_48(unsigned voff, const void* sbase, unsigned ldsaddr) {
    asm volatile("s_mov_b32 m0, %2\n\ts_mov_b32 exec_hi, 0xffff\n\tglobal_load_lds_dwordx4 %0, %1" NT_IN "\n\ts_mov_b32 exec_hi, -1" :: "v"(voff), "s"(sbase), "s"(ldsaddr) : "memory");
}
DI void glds_48b(unsigned voff, const void* sbase, unsigned ldsaddr) {
    asm volatile("s_mov_b32 m0, %2\n\ts_mov_b32 exec_hi, 0xffff\n\tglobal_load_lds_dwordx4 %0, %1" NT_W2 "\n\ts_mov_b32 exec_hi, -1" :: "v"(voff), "s"(sbase), "s"(ldsaddr) : "memory");
}
DI void glds_m(unsigned voff, const void* sbase, unsigned ldsaddr, unsigned long long mask) {
    asm volatile("s_mov_b32 m0, %2\n\ts_mov_b64 exec, %3\n\tglobal_load_lds_dwordx4 %0, %1" NT_IN "\n\ts_mov_b64 exec, -1" :: "v"(voff), "s"(sbase), "s"(ldsaddr), "s"(mask) : "memory");
}
DI void glds_m_nt(unsigned voff, const void* sbase, unsigned ldsaddr, unsigned long long mask) {
    asm volatile("s_mov_b32 m0, %2\n\ts_mov_b64 exec, %3\n\tglobal_load_lds_dwordx4 %0, %1" NT_KV "\n\ts_mov_b64 exec, -1" :: "v"(voff), "s"(sbase), "s"(ldsaddr), "s"(mask) : "memory");
}
DI void glds_poll(unsigned voff, const void* sbase, unsigned ldsaddr) {
    asm volatile("s_mov_b32 m0, %2\n\ts_mov_b64 exec, 0xff\n\tglobal_load_lds_dword %0, %1 sc1\n\ts_mov_b64 exec, -1" :: "v"(voff), "s"(sbase), "s"(ldsaddr) : "memory");
}
DI void glds_dw(unsigned voff, const void* sbase, unsigned ldsaddr) {
    asm volatile("s_mov_b32 m0, %2\n\ts_nop 0\n\tglobal_load_lds_dword %0, %1" :: "v"(voff), "s"(sbase), "s"(ldsaddr) : "memory");
}
DI void glds_m16(unsigned voff, const void* sbase, unsigned ldsaddr) {
    asm volatile("s_mov_b32 m0, %2\n\ts_mov_b64 exec, 0xffff\n\tglobal_load_lds_dword %0, %1" NT_T "\n\ts_mov_b64 exec, -1" :: "v"(voff), "s"(sbase), "s"(ldsaddr) : "memory");
}
DI void gstore16_sc1(unsigned voff, void* sbase, u32x4 d) { asm volatile("global_store_dwordx4 %0, %1, %2 sc1" NT_ST "\n\ts_nop 1" :: "v"(voff), "v"(d), "s"(sbase) : "memory"); }
DI void gstore8_sc1(unsigned voff, void* sbase, u32x2 d) { asm volatile("global_store_dwordx2 %0, %1, %2 sc1" :: "v"(voff), "v"(d), "s"(sbase) : "memory"); }
DI void gstore4_sc1_m(unsigned voff, void* sbase, unsigned d, unsigned long long mask) {
    asm volatile("s_mov_b64 exec, %3\n\tglobal_store_dword %0, %1, %2 sc1\n\ts_mov_b64 exec, -1" :: "v"(voff), "v"(d), "s"(sbase), "s"(mask) : "memory");
}
template <class T> DI void gload16_sc1(T& d, unsigned voff, const void* sbase) { asm volatile("global_load_dwordx4 %0, %1, %2 sc1" : "=v"(d) : "v"(voff), "s"(sbase) : "memory"); }
DI void gload8_a(u32x2& d, unsigned voff, const void* sbase) { asm volatile("global_load_dwordx2 %0, %1, %2" : "=v"(d) : "v"(voff), "s"(sbase) : "memory"); }
DI void gload2_sc1(unsigned& d, unsigned voff, const void* sbase) { asm volatile("global_load_ushort %0, %1, %2 sc1" : "=v"(d) : "v"(voff), "s"(sbase) : "memory"); }
DI s16x4 trread(unsigned a) { return __builtin_bit_cast(s16x4, __builtin_amdgcn_ds_read_tr16_b64_v4i16((LDSP v4i16_t*)(uintptr_t)a)); }
DI bf16x8 trfrag(unsigned a, int o0, int o1) {
    s16x4 lo = trread(a + o0), hi = trread(a + o1);
    return __builtin_shufflevector(lo, hi, 0, 1, 2, 3, 4, 5, 6, 7);
}
DI bf16x8 ldsread16(unsigned a) { return *(const LDSP bf16x8*)(uintptr_t)a; }
DI s16x4 ldsread8v(unsigned a) { return *(const volatile LDSP s16x4*)(uintptr_t)a; }
DI unsigned ldsread4(unsigned a) { return *(const volatile LDSP unsigned*)(uintptr_t)a; }
#define CAT8(lo, hi) __builtin_shufflevector(lo, hi, 0, 1, 2, 3, 4, 5, 6, 7)
#define MFMA32(a, b, c) __builtin_amdgcn_mfma_f32_32x32x16_bf16(a, b, c, 0, 0, 0)
#define MFMA16(a, b, c) __builtin_amdgcn_mfma_f32_16x16x32_bf16(a, b, c, 0, 0, 0)
#define STR_(x) #x
#define WAITV_BAR(N) asm volatile("s_waitcnt vmcnt(" STR_(N) ")\n\ts_barrier" ::: "memory")
#define MFMA16A(c, a, b) asm volatile("v_mfma_f32_16x16x32_bf16 %0, %1, %2, %0" : "+v"(c) : "v"(a), "v"(b))
#define MFMA16Z(c, a, b) asm volatile("v_mfma_f32_16x16x32_bf16 %0, %1, %2, 0" : "=&v"(c) : "v"(a), "v"(b))
template <int V> struct IC { static constexpr int value = V; };
template <int I, int N, class F> DI void static_for(F&& f) { if constexpr (I < N) { f(IC<I>{}); static_for<I + 1, N>(f); } }
#define SFOR(var, lo, hi) static_for<lo, hi>([&](auto var##_c) __attribute__((always_inline)) { constexpr int var = decltype(var##_c)::value;
#define SEND });
DI bf16x8 shr1z(bf16x8 v) {
    u32x4 a = __builtin_bit_cast(u32x4, v), r;
#pragma unroll
    for (int e = 0; e < 4; ++e) r[e] = __builtin_amdgcn_update_dpp(0, a[e], 0x111, 0xf, 0xf, true);
    return __builtin_bit_cast(bf16x8, r);
}
constexpr size_t WS_H = 0, WS_INBOX = 20u << 20, WS_KVR = 40u << 20, WS_Y2T = 44u << 20, WS_SYNC = 48u << 20;
constexpr int SY_NONCE = 0, SY_ECHO = 1024, SY_CNT = 4096;
constexpr int CN_PREP = 0, CN_CONV = 8, CN_N = 12, SY_HF = 8192;
constexpr int INBOX_B = 27648;
constexpr unsigned SPIN_CAP = 1u << 15;
#define CODE_BYTES 36000
#define X_H 1
#define X_W 1
#define X_T 1
#define LOOPN 12
#define BVN "12"
#define XP 1
#define XI 0
#define Y_G 1
#define Y_P 1
#define Y_L true
#define X_GATE_EXACT 1
#define TAILMODE 1
constexpr int G1_BLK = 2432, G1_WMAT = 9616, G1_WSLOT = 19216, G1_XOFF = 96128, L_MISC = G1_XOFF + 65536, L_TOTAL = 163840;
constexpr int MI_GW = L_MISC, MI_POLL = L_MISC + 1152, MI_ARR = L_MISC + 1216, MI_G = L_MISC + 1280, MI_DUMMY = L_MISC + 1536;
constexpr int CV_HC = 4160, CV_U = 4 * CV_HC, CV_CH = CV_U + 47 * 160 + 32, CV_BASE = L_MISC - 3 * CV_CH - 9216, CV_RED = CV_BASE + 3 * CV_CH;
constexpr int G2_STG = 16 * G1_BLK;
static_assert(CV_BASE >= 2 * G2_STG && CV_BASE >= 3 * G1_WSLOT && CV_BASE % 64 == 0 && 4 * G2_STG <= L_MISC, "lds map");
constexpr int FN_X = G2_STG;

DI void cnt_add(char* sync, int c) {
    unsigned one = 1, off = SY_CNT + c * 256;
    asm volatile("s_mov_b64 exec, 1\n\tglobal_atomic_add %0, %1, %2 sc1\n\ts_mov_b64 exec, -1" :: "v"(off), "v"(one), "s"(sync) : "memory");
}
DI void spin_wait(char* sync, unsigned voff, unsigned lo, unsigned span, unsigned noecho, unsigned vlin) {
    unsigned cnt, t; u32x4 e;
    asm volatile(
        "s_mov_b32 %0, 0x8000\n"
        "1:\n\t"
        "s_cmp_lg_u32 %8, 0\n\t"
        "s_cbranch_scc1 2f\n\t"
        "global_load_dwordx4 %2, %7, %4 sc1\n\t"
        "s_waitcnt vmcnt(0)\n\t"
        "global_store_dwordx4 %7, %2, %4 offset:1024 sc1\n"
        "2:\n\t"
        "global_load_dword %1, %3, %4 sc1\n\t"
        "s_waitcnt vmcnt(0)\n\t"
        "v_subrev_u32 %1, %5, %1\n\t"
        "v_cmp_ge_u32 vcc, %6, %1\n\t"
        "s_cmp_eq_u64 vcc, -1\n\t"
        "s_cbranch_scc1 3f\n\t"
        "s_sleep 4\n\t"
        "s_sub_u32 %0, %0, 1\n\t"
        "s_cbranch_scc0 1b\n"
        "3:"
        : "=&s"(cnt), "=&v"(t), "=&v"(e) : "v"(voff), "s"(sync), "s"(lo), "s"(span), "v"(vlin), "s"(noecho) : "memory", "vcc", "scc");
}
constexpr unsigned GE = 0x7fffffffu;
DI void conv_out(f32x4 acc, unsigned ubase, int m, int n, int lane, float dD, u16* yrow) {
    const int I = 16 * n + (lane & 15), tl = 16 * m + 4 * (lane >> 4);
    u32x2 uu = *(const LDSP u32x2*)(uintptr_t)(ubase + (31 - I) * 160 + (60 - tl) * 2);
    float kv[4] = {bfhi(uu[1]), bflo(uu[1]), bfhi(uu[0]), bflo(uu[0])};
    float o[4];
#pragma unroll
    for (int r = 0; r < 4; ++r) o[r] = rbf(acc[r]) + rbf(kv[r] * dD);
    u32x2 w = {pk2bf(o[0], o[1]), pk2bf(o[2], o[3])};
    gstore8_sc1((64 * I + tl) * 2, yrow, w);
}
DI unsigned nosent(unsigned x) { return x < 0xfffffffeu ? x : 0xfffffffeu; }

#if defined(__HIP_DEVICE_COMPILE__)
#define NOPK
#else
#define NOPK
#endif
__global__ NOPK __launch_bounds__(512, 2) void k_mega(const u16* __restrict__ q, const u16* __restrict__ kk_, const u16* __restrict__ vv_, const u16* __restrict__ sk,
                                                      const u16* __restrict__ hk, const u16* __restrict__ sD, const u16* __restrict__ w1, const u16* __restrict__ w3,
                                                      const u16* __restrict__ w2, const u16* __restrict__ gw, u16* __restrict__ out, char* __restrict__ ws DBG_PARAM) {
    extern __shared__ __attribute__((aligned(16))) char smem[];
    unsigned long long tA = 0, tB = 0;
    DBG_STAMP(30);
    const char* pc0 = (const char*)__builtin_amdgcn_s_getpc();
    int tid = __builtin_amdgcn_workitem_id_x(), lane = tid & 63;
    {
        if (tid < 64) glds_dw(tid * 128, pc0, (unsigned)(uintptr_t)smem + MI_DUMMY);
    }
    SBAR();
    asm volatile("" :: "s"(q), "s"(kk_), "s"(vv_), "s"(sk), "s"(hk), "s"(sD), "s"(w1), "s"(w3), "s"(w2), "s"(gw), "s"(out), "s"(ws));
    DBG_STAMP(0);
    const int wid = __builtin_amdgcn_readfirstlane(tid >> 6);
    const int wm = wid & 3, wf = wid >> 2, kh = wf;
    const int bid = __builtin_amdgcn_workgroup_id_x(), grp = bid & 7, idx = bid >> 3;
    const unsigned lds0 = (unsigned)(uintptr_t)smem;
    char* sync = ws + WS_SYNC;
    const int mt1 = (grp & 1) * 4 + (idx & 3), ft = (grp >> 1) * 8 + (idx >> 2);
    const int mt2 = mt1 * 2 + ((idx >> 2) & 1), nt = idx >> 3, ks = grp >> 1;
    const int ptt = grp * 4 + (idx >> 3), pdt = idx & 7;
    const int quad = nt * 16 + mt2, ch0 = quad * 9 + 2 * ks, ch8 = quad * 9 + 8;
    const unsigned vlin = lane * 16;

    u32x2 skv[2];
    {
        const int t0 = ptt * 64;
#pragma unroll
        for (int j = 0; j < 3; ++j) {
            const int p = wid + 8 * j;
            if (p < 19) {
                const bool isk = p >= 9; const int c = (isk ? p - 9 : p) * 64 + lane;
                int row = c / 9; const int cc = c - row * 9; row += t0 - (isk ? 3 : 0); row = row < 0 ? 0 : row;
                glds_m_nt(row * (D_ * 2) + cc * 16, (const char*)(isk ? kk_ : vv_) + pdt * 144, lds0 + 4 * G1_WSLOT + p * 1024, p == 18 ? ((1ull << 27) - 1) : ~0ull);
            }
        }
        gload8_a(skv[0], (lane & 7) * 8, (const char*)sk + pdt * 576 + wid * 64);
        gload8_a(skv[1], 0u, (const char*)sk + pdt * 576 + 512 + wid * 8);
        if (wid == 0) {
            glds_poll(bid * 4, sync + SY_ECHO, lds0 + MI_POLL);
            if (bid == 0) gstore4_sc1_m(SY_CNT + lane * 256, sync, 0u, (1ull << CN_N) - 1);
        }
        if (bid == 0 && wid >= 4) gstore4_sc1_m(SY_HF + (tid - 256) * 128, sync, 0u, ~0ull);
        if (tid == 64 || tid == 65) *(LDSP u32x4*)(uintptr_t)(lds0 + MI_ARR + (tid & 1) * 16) = u32x4{0u, 0u, 0u, 0u};
        if (wid == 7) { glds_full(vlin, gw, lds0 + MI_GW); glds_m(vlin, (const char*)gw + 1024, lds0 + MI_GW + 1024, 0xffull); }
    }
    const int fodd = ft & 1;
    unsigned voffW[3], voff2[3];
#pragma unroll
    for (int i = 0; i < 3; ++i) {
        const int c = i * 48 + (lane < 48 ? lane : 0), r = c / 18, c0 = c - r * 18; int cc = c0 + 2 * fodd; cc = cc >= 18 ? cc - 18 : cc;
        voffW[i] = r * (F_ * 2) + cc * 16; voff2[i] = r * (D_ * 2) + c0 * 16;
    }
    const char* wsrc = (const char*)(wf ? w3 : w1) + ft * 288 + (size_t)wm * (8 * F_ * 2);
    const unsigned wdst = lds0 + wf * G1_WMAT + wm * G1_BLK;
    unsigned voffX[2];
#pragma unroll
    for (int i = 0; i < 2; ++i) { int r = 8 * i + (lane >> 3); voffX[i] = r * (D_ * 2) + (((lane & 7) ^ ((r >> 1) & 7)) * 16); }
    const char* xsrc = (const char*)q + (size_t)(mt1 * 256 + wid * 32) * (D_ * 2);
    const unsigned xdst = lds0 + G1_XOFF + wid * 4096;
#define G1_W(u, slotoff, i) glds_48(voffW[i], wsrc + (size_t)(u) * (32 * F_ * 2), wdst + (slotoff) + (i) * 768)
#define G1_X(s, i) glds_full(voffX[(i) & 1], xsrc + (s) * 128 + ((i) >> 1) * (16 * D_ * 2), xdst + ((s) & 1) * 32768 + (i) * 1024)
    G1_X(0, 0); G1_X(0, 1); G1_X(0, 2); G1_X(0, 3);
    G1_W(0, 0, 0); G1_W(0, 0, 1); G1_W(0, 0, 2);
    G1_W(1, G1_WSLOT, 0); G1_W(1, G1_WSLOT, 1); G1_W(1, G1_WSLOT, 2);
    G1_X(1, 0); G1_X(1, 1); G1_X(1, 2); G1_X(1, 3);
    G1_W(2, 2 * G1_WSLOT, 0); G1_W(2, 2 * G1_WSLOT, 1); G1_W(2, 2 * G1_WSLOT, 2);
    if (wid == 1 + (idx & 3)) { unsigned o = tid * 128; o = o < CODE_BYTES ? o : 0; glds_dw(o, pc0, lds0 + MI_DUMMY); }
    const unsigned rW = lds0 + (lane >> 4) * G1_BLK + ((lane & 15) >> 2) * 288 + (lane & 3) * 8;
    const unsigned rX = xdst + (lane & 15) * 128 + (((lane >> 4) ^ ((lane >> 1) & 7)) * 16);
#define G1_AF(base, i) trfrag(base, ((i) & 1) * G1_WMAT + ((i) >> 1) * 32, ((i) & 1) * G1_WMAT + ((i) >> 1) * 32 + 4 * 288)
#define G1_AH(base, i, t) trread((base) + ((i) & 1) * G1_WMAT + ((i) >> 1) * 32 + (t) * 4 * 288)
#define G1_XF(u, m) ldsread16((rX ^ (((u) & 1) * 64)) + (((u) >> 1) & 1) * 32768 + (m) * 2048)
    f32x4 acc[18][2];
#pragma unroll
    for (int i = 0; i < 18; ++i) { acc[i][0] = f32x4{}; acc[i][1] = f32x4{}; }
    s16x4 al[6], ah[6]; bf16x8 xb[2][2];
    const int cci = wm >> 1, cch = ch0 + cci;
    const unsigned ccb = lds0 + CV_BASE + cci * CV_CH;
    unsigned voffT = ((lane & 15) < (wid < 6 ? 9 : (wid == 6 ? 11 : 6)) ? (lane & 15) : 0) * 128;
    const char* tsrc = wid < 6 ? (const char*)w2 + (size_t)(ks * 1152 + idx * 36 + wid * 6) * (D_ * 2) : (const char*)hk + (size_t)(wid == 6 ? ch0 : ch8) * (S_ * 2);
    const int tstep = wid < 6 ? D_ * 2 : (wid == 6 ? 1344 : 640);
    LAUNDER(voffT);
    unsigned cvA, cvB, voffU[2], zaddr, baddr; int boff;
    {
        const int ap = lane & 15, g = lane >> 4;
        cvA = ccb + ((ap + 1) & 3) * CV_HC + 2 * ((ap + 1) & ~3) + 16 * g + 32 * (wm & 1);
        cvB = ccb + CV_U + (15 - ap) * 160 + 16 * g;
#pragma unroll
        for (int z = 0; z < 2; ++z) {
            int up = wid * 2 + z; up = up > 14 ? 14 : up;
            const int cidx = (up % 5) * 64 + lane, row = cidx / 10, cc = cidx - row * 10;
            voffU[z] = (up >= 10 ? ch8 : ch0 + up / 5) * (S_ * 2) + row * 128 + (cc < 8 ? cc : 0) * 16; LAUNDER(voffU[z]);
        }
        const int tz = tid < 3 * 162 ? tid : 0, ci = tz / 162, zf = tz - ci * 162;
        zaddr = lds0 + CV_BASE + ci * CV_CH + (zf < 12 ? (zf / 3) * CV_HC + (zf % 3) * 16 : CV_U + 32 * 160 + (zf - 12) * 16); LAUNDER(zaddr);
        const int c = lane >> 4, which = (lane >> 3) & 1, jx = lane & 7, ix = (which ? 2040 : -8) + c + jx;
        baddr = lds0 + CV_BASE + (wid > 2 ? 2 : wid) * CV_CH + c * CV_HC + (which ? 259 : 3) * 16 + jx * 2; LAUNDER(baddr);
        boff = ix < 0 ? -2 : (ix > S_ - 1 ? -2 : ix * 2); LAUNDER(boff);
        LAUNDER(cvA); LAUNDER(cvB); LAUNDER(voff2[0]); LAUNDER(voff2[1]); LAUNDER(voff2[2]);
    }
    unsigned sdw, sdw8;
    asm volatile("s_load_dword %0, %2, 0x0\n\ts_load_dword %1, %3, 0x0\n\ts_waitcnt lgkmcnt(0)" : "=&s"(sdw), "=&s"(sdw8) : "s"((const char*)sD + (cch >> 1) * 4), "s"((const char*)sD + (ch8 >> 1) * 4) : "memory");
    SBAR();
    asm volatile("s_waitcnt vmcnt(10)\n\ts_barrier" : "+v"(skv[0]), "+v"(skv[1]) :: "memory");
    SBAR();
    DBG_STAMP(1);
    unsigned nonce = 0;
    u32x2 kr[3], vr[2]; unsigned ke[5], pw[5]; float kf[16], sc[8], po[9], px, py;
    const int poct = 2 * (lane >> 4) + ((lane >> 3) & 1);
    char* kvb = ws + WS_KVR + (size_t)(pdt * 72) * (S_ * 2) + (S_ - 64 - ptt * 64) * 2;
    unsigned pa = lds0 + 4 * G1_WSLOT + (8 * (2 * (lane >> 4) + ((lane & 3) >> 1)) + ((lane & 15) >> 2)) * 144 + (lane & 1) * 8 + wid * 16;
    unsigned pe = lds0 + 4 * G1_WSLOT + lane * 144 + 128 + wid * 2;
    unsigned pso = (wid * 8 + (lane & 7)) * (S_ * 2) + (7 - poct) * 16;
    unsigned pm0 = (ptt == 0 && poct == 0) ? 0u : ~0u;
#define PREP_GAP(g) do { \
        if ((g) == 0) { kr[0] = __builtin_bit_cast(u32x2, trread(pa + 9216)); kr[1] = __builtin_bit_cast(u32x2, trread(pa + 9216 + 576)); } \
        if ((g) == 1) { kr[2] = __builtin_bit_cast(u32x2, trread(pa + 9216 + 1152)); vr[0] = __builtin_bit_cast(u32x2, trread(pa)); } \
        if ((g) == 2) { vr[1] = __builtin_bit_cast(u32x2, trread(pa + 576)); ke[4] = *(const LDSP u16*)(uintptr_t)pe; } \
        if ((g) == 3 || (g) == 4) { ke[2 * ((g) - 3)] = *(const LDSP u16*)(uintptr_t)(pe + 9216 + 2 * ((g) - 3) * 144); ke[2 * ((g) - 3) + 1] = *(const LDSP u16*)(uintptr_t)(pe + 9216 + (2 * ((g) - 3) + 1) * 144); } \
        if ((g) < 4) { const unsigned w_ = skv[((g) >> 1) & 1][(g) & 1]; sc[2 * ((g) & 3)] = bflo(w_); sc[2 * ((g) & 3) + 1] = bfhi(w_); LAUNDER(sc[2 * ((g) & 3)]); LAUNDER(sc[2 * ((g) & 3) + 1]); } \
        if ((g) == 8) { kr[0][0] &= pm0; kr[0][1] &= pm0 | 0xffff0000u; LAUNDER(kr[0]); } \
        if ((g) >= 9 && (g) < 13) { _Pragma("unroll") for (int e_ = 0; e_ < 3; ++e_) { const int n_ = (((g) - 9) & 3) * 3 + e_; const unsigned w_ = kr[n_ >> 2][(n_ >> 1) & 1]; \
                kf[n_] = (n_ & 1) ? bfhi(w_) : bflo(w_); LAUNDER(kf[n_]); } } \
        if ((g) == 13 || (g) == 14) { _Pragma("unroll") for (int e_ = 0; e_ < 2; ++e_) { const int n_ = (((g) - 13) & 1) * 2 + e_; \
                kf[12 + n_] = (n_ < 3 && ptt == 0 && lane + n_ < 3) ? 0.f : bflo(ke[n_]); LAUNDER(kf[12 + n_]); } } \
        if ((g) >= 15 && (g) < 69) { const int t_ = (((g) - 15) / 6) % 9, m_ = ((g) - 15) % 6, kb_ = t_ < 8 ? t_ : 12, sb_ = t_ < 8 ? 0 : 4; \
            if (m_ == 0) { px = rbf(kf[kb_] * sc[sb_ + 3]); py = kf[kb_ + 1] * sc[sb_ + 2]; } \
            if (m_ == 1) { px = rbf(px + rbf(py)); } \
            if (m_ == 2) { py = rbf(kf[kb_ + 2] * sc[sb_ + 1]); px += py; } \
            if (m_ == 3) { px = rbf(px); py = rbf(kf[kb_ + 3] * sc[sb_]); } \
            if (m_ == 4) { px = rbf(px + py); } \
            if (m_ == 5) { const unsigned w_ = t_ < 8 ? vr[(t_ >> 2) & 1][(t_ >> 1) & 1] : ke[4]; po[t_] = px * ((t_ & 1) ? bfhi(w_) : bflo(w_)); LAUNDER(po[t_]); \
                           if (t_ & 1) { pw[3 - (t_ >> 1)] = pk2bf(po[t_], po[t_ - 1]); LAUNDER(pw[3 - (t_ >> 1)]); } } \
            LAUNDER(px); LAUNDER(py); } \
        if ((g) == 69) gstore16_sc1(pso, kvb, u32x4{pw[0], pw[1], pw[2], pw[3]}); \
        if ((g) == 70) asm volatile("global_store_short %0, %1, %2 sc1" :: "v"((63 - lane) * 2), "v"(pk2bf(po[8], 0.f)), "s"(kvb + (64 + wid) * (S_ * 2)) : "memory"); \
    } while (0)
    if (wid == 0) { nonce = __builtin_amdgcn_readfirstlane(ldsread4(lds0 + MI_POLL)) + 1u; gstore4_sc1_m(SY_NONCE + bid * 4, sync, nonce, 1ull); }
    SBAR();
    al[0] = G1_AH(rW, 0, 0); ah[0] = G1_AH(rW, 0, 1); xb[0][0] = G1_XF(0, 0);
    G1_W(3, 3 * G1_WSLOT, 0);
    SBAR();
    al[1] = G1_AH(rW, 1, 0); ah[1] = G1_AH(rW, 1, 1); xb[0][1] = G1_XF(0, 1);
    G1_W(3, 3 * G1_WSLOT, 1);
    SBAR();
    al[2] = G1_AH(rW, 2, 0); ah[2] = G1_AH(rW, 2, 1);
    G1_W(3, 3 * G1_WSLOT, 2);
    SBAR();
    al[3] = G1_AH(rW, 3, 0); ah[3] = G1_AH(rW, 3, 1);
    SBAR();
    char* inbox = ws + WS_INBOX + (size_t)bid * INBOX_B;
    int so_cur = 0;
    float gacc = 0.f, gt_; u32x2 gq, gg_;
    const unsigned gtW = lds0 + MI_GW + (lane & 15) * 8;
    const unsigned gtX = lds0 + G1_XOFF + ((mt2 & 1) * 4 + ks) * 4096 + (wid * 4 + (lane >> 4)) * 128 + ((((lane & 15) >> 1) ^ (((wid * 4 + (lane >> 4)) >> 1) & 7)) * 16) + (lane & 1) * 8;
#define GATE_ACC() do { gacc += (rbf(bflo(gq[0]) * bflo(gg_[0])) + rbf(bfhi(gq[0]) * bfhi(gg_[0]))) + (rbf(bflo(gq[1]) * bflo(gg_[1])) + rbf(bfhi(gq[1]) * bfhi(gg_[1]))); LAUNDER(gacc); } while (0)
#define GATE_E(e, h) do { if ((h) == 0) { gt_ = (((e) & 1) ? bfhi(gq[((e) >> 1) & 1]) : bflo(gq[((e) >> 1) & 1])) * (((e) & 1) ? bfhi(gg_[((e) >> 1) & 1]) : bflo(gg_[((e) >> 1) & 1])); LAUNDER(gt_); } \
                          else { gacc += rbf(gt_); LAUNDER(gacc); } } while (0)
#define G1_PROTO(u) do { \
    if (bid == 0 && (u) >= 2) {     \
        const unsigned pad_ = lds0 + (wid / 6) * G1_WSLOT + ((wid % 6) / 3) * G1_WMAT + (wid % 3) * G1_BLK + 2304; \
        if ((u) >= 4) gstore4_sc1_m(SY_ECHO + wid * 128 + (vlin >> 2), sync, ldsread4(pad_ + ((vlin >> 2) & 127)), 0xffffffffull); \
        if ((u) <= 10) asm volatile("s_mov_b32 m0, %2\n\ts_mov_b32 exec_hi, 0\n\tglobal_load_lds_dword %0, %1 sc1\n\ts_mov_b32 exec_hi, -1" :: "v"(vlin >> 2), "s"(sync + SY_NONCE + wid * 128), "s"(pad_) : "memory"); } \
    if (wid == 0) { \
        if ((u) == 6) { glds_poll(bid * 4, sync + SY_ECHO, lds0 + MI_POLL); } \
        else if ((u) == 8) { \
            if (__builtin_amdgcn_readfirstlane(ldsread4(lds0 + MI_POLL)) != nonce) { DBG_STAMP(18); spin_wait(sync, SY_ECHO + bid * 4, nonce, 0u, bid, vlin); } \
            cnt_add(sync, CN_PREP + pdt); } \
        else if ((u) == 12) { glds_poll((vlin & 0x70) * 16, sync + SY_CNT + CN_PREP * 256, lds0 + MI_POLL); } \
    } } while (0)
#define G1_HOOK(u, i)
#define G1_PREBAR(u)
#define G1_SUB(u, P, WN, GL, NEXT, PEEL) do { \
        const int so_nxt = so_cur == 4 * G1_WSLOT ? 0 : so_cur + G1_WSLOT; \
        const int so_prv = so_cur == 0 ? 4 * G1_WSLOT : so_cur - G1_WSLOT; \
        const unsigned aC = rW + so_cur, aN = rW + so_nxt; \
        _Pragma("unroll") for (int i = 0; i < 18; ++i) { \
            if (i == 14 && (NEXT)) { G1_PREBAR(u); WAITV_BAR(WN); SBAR(); if (Y_P && GL && P == 0) { G1_PROTO(u); SBAR(); } } \
            acc[i][0] = MFMA16(CAT8(al[i % 6], ah[i % 6]), xb[P][0], acc[i][0]); \
            SBAR(); \
            if (i + 4 < 18) al[(i + 4) % 6] = G1_AH(aC, i + 4, 0); \
            else if (NEXT) al[(i + 4) % 6] = G1_AH(aN, i + 4 - 18, 0); \
            if (PEEL) PREP_GAP(P * 36 + 2 * i); \
            if (Y_G && P == 0 && i >= 8 && i < 12) GATE_E(i - 8, 0); \
            SBAR(); \
            acc[i][1] = MFMA16(CAT8(al[i % 6], ah[i % 6]), xb[P][1], acc[i][1]); \
            SBAR(); \
            if (i + 4 < 18) ah[(i + 4) % 6] = G1_AH(aC, i + 4, 1); \
            else if (NEXT) ah[(i + 4) % 6] = G1_AH(aN, i + 4 - 18, 1); \
            if (PEEL) PREP_GAP(P * 36 + 2 * i + 1); \
            if (Y_G && P == 0 && i >= 8 && i < 12) GATE_E(i - 8, 1); \
            if (P == 0 && (i == 1 || i == 3)) xb[1][i >> 1] = G1_XF((u) + 1, i >> 1); \
            if (P == 1 && (NEXT) && (i == 14 || i == 15)) xb[0][i - 14] = G1_XF((u) + 1, i - 14); \
            SBAR(); \
            G1_HOOK(u, i); \
            if (Y_G && P == 0 && i == 5) { gq = *(const LDSP u32x2*)(uintptr_t)(gtX + (((u) >> 1) & 1) * 32768); gg_ = *(const LDSP u32x2*)(uintptr_t)(gtW + ((u) >> 1) * 128); SBAR(); } \
            if (GL && i >= 14 && i <= 16) G1_W((u) + 4, so_prv, i - 14); \
            if (X_T == 1 && !(PEEL) && GL && P == 0 && i == 17) { glds_m16(voffT, tsrc, lds0 + MI_DUMMY); tsrc += tstep; } \
            if (GL && P == XP && i >= XI && i < XI + 8 && (i & 1) == 0) G1_X(((u) >> 1) + 2, (i - XI) >> 1); \
            if (GL && P == 1 && i == 17) { int pc_ = ((u) >> 1) * 8 + wid; pc_ = pc_ > 53 ? 53 : pc_; u32x4 t_;     \
                asm volatile("v_mov_b32 %0, -1\n\tv_mov_b32 %1, -1\n\tv_mov_b32 %2, -1\n\tv_mov_b32 %3, -1" : "=v"(t_[0]), "=v"(t_[1]), "=v"(t_[2]), "=v"(t_[3])); \
                asm volatile("s_mov_b32 exec_hi, 0\n\tglobal_store_dwordx4 %0, %1, %2 sc1\n\ts_mov_b32 exec_hi, -1\n\ts_nop 0" :: "v"(vlin), "v"(t_), "s"(inbox + pc_ * 512) : "memory"); } \
            SBAR(); \
        } \
        so_cur = so_nxt; \
    } while (0)
    G1_SUB(0, 0, 10, true, true, true);
    SBAR();
    G1_SUB(1, 1, 10, true, true, true);
    DBG_STAMP(8);
    for (int u = 2; u < 14; u += 2) {
        G1_SUB(u, 0, LOOPN, Y_L, true, false);
        G1_SUB(u + 1, 1, LOOPN, Y_L, true, false);
    }
    DBG_STAMP(9);
    LAUNDER(lane);
    unsigned bval;
    const char* hsrcA = (const char*)hk + (size_t)ch0 * (S_ * 2) + wm * 2 + wf * 2048; const char* hsrcB = (const char*)hk + (size_t)ch8 * (S_ * 2) + wm * 2 + wf * 2048;
    const unsigned hdst = lds0 + CV_BASE + wm * CV_HC + 64 + wf * 2048;
    const unsigned hmask = wf ? 0x7fffffffu : ~0u;
#undef G1_HOOK
#undef G1_PREBAR
#define CV_STAGE(z) do { \
        if ((z) < 6) asm volatile("s_add_u32 m0, %2, %3\n\ts_mov_b32 exec_hi, %4\n\tglobal_load_lds_dwordx4 %0, %1 offset:%5\n\ts_mov_b32 exec_hi, -1" \
                         :: "v"(vlin), "s"((z) >= 4 ? hsrcB : ((z) >= 2 ? hsrcA + S_ * 2 : hsrcA)), "s"(hdst), "i"(((z) >> 1) * CV_CH), "s"(((z) & 1) ? hmask : ~0u), "i"(((z) & 1) * 1024) : "memory", "scc"); \
        else { int up_ = wid * 2 + ((z) & 1); up_ = up_ > 14 ? 14 : up_; const int ci_ = up_ / 5; glds_sc1(voffU[(z) & 1], ws + WS_KVR, lds0 + CV_BASE + ci_ * CV_CH + CV_U + (up_ - ci_ * 5) * 1024); } \
        SBAR(); } while (0)
#define G1_HOOK(u, i) do { \
        if ((u) == 15 && i == 2) gload2_sc1(bval, boff < 0 ? 0 : boff, (const char*)hk + (size_t)(wid >= 2 ? ch8 : ch0 + wid) * (S_ * 2)); \
        if ((u) == 16 && i == 15) CV_STAGE(0); if ((u) == 16 && i == 17) CV_STAGE(1); \
        if ((u) == 17 && i == 1) CV_STAGE(2); if ((u) == 17 && i == 3) CV_STAGE(3); if ((u) == 17 && i == 5) CV_STAGE(4); if ((u) == 17 && i == 7) CV_STAGE(5); \
        if ((u) == 17 && i == 9) CV_STAGE(6); if ((u) == 17 && i == 11) CV_STAGE(7); \
        if ((u) == 17 && i == 13) { asm volatile("" : "+v"(bval)); *(LDSP u32x4*)(uintptr_t)zaddr = u32x4{0, 0, 0, 0}; *(LDSP u16*)(uintptr_t)baddr = boff < 0 ? (u16)0 : (u16)bval; SBAR(); } \
    } while (0)
#define G1_PREBAR(u) do { if ((u) == 16 && wid == 0) {     \
            const unsigned sh_ = bid == 0 ? (vlin >> 4) & 7 : quad >> 3; \
            if (!wave_all(ldsread4(lds0 + MI_POLL + sh_ * 4) >= 32u)) { DBG_STAMP(19); spin_wait(sync, SY_CNT + (CN_PREP + sh_) * 256, 32u, GE, bid, vlin); } } } while (0)
    G1_SUB(14, 0, LOOPN, false, true, false);
    G1_SUB(15, 1, 5, false, true, false);
    G1_SUB(16, 0, 0, false, true, false);
    G1_SUB(17, 1, 0, false, false, false);
    asm volatile("s_barrier" ::: "memory");
    DBG_STAMP(2);
    const char* w2src = (const char*)w2 + (size_t)(ks * 1152 + kh * 576 + wm * 16) * (D_ * 2) + nt * 288;
    const unsigned w2dst = lds0 + (kh * 8 + wm * 2) * G1_BLK;
#define G2_W(s, i) glds_48b(voff2[(i) % 3], w2src + (size_t)(s) * (64 * D_ * 2) + ((i) / 3) * (8 * D_ * 2), w2dst + ((s) & 3) * G2_STG + ((i) / 3) * G1_BLK + ((i) % 3) * 768)
    {
        gacc += asf(__builtin_amdgcn_update_dpp(0, __builtin_bit_cast(int, gacc), 0xB1, 0xf, 0xf, true));
        gacc += asf(__builtin_amdgcn_update_dpp(0, __builtin_bit_cast(int, gacc), 0x4E, 0xf, 0xf, true));
        gacc += asf(__builtin_amdgcn_update_dpp(0, __builtin_bit_cast(int, gacc), 0x141, 0xf, 0xf, true));
        gacc += asf(__builtin_amdgcn_update_dpp(0, __builtin_bit_cast(int, gacc), 0x140, 0xf, 0xf, true));
        *(LDSP float*)(uintptr_t)(lds0 + MI_G + (wid * 4 + (lane >> 4)) * 4) = gacc;
        char* hb = ws + WS_H + ((size_t)(mt1 * 16 + wid * 2) * 144 + ((9 * ft + 1) >> 1)) * 1024;
        const unsigned hso = ((lane >> 4) & 1) * (144 * 1024) + ((2 * fodd + (lane >> 5)) * 16 + (lane & 15)) * 16;
        unsigned pk[2][4];
        float nl2e = -1.4426950408889634f; LAUNDER_S(nl2e);
        SBAR();
        SFOR(j, 0, 9)
            f32x2 T[4], P[4];
#define SW2(a, e) f32x2{acc[a][(e) >> 1][2 * ((e) & 1)], acc[a][(e) >> 1][2 * ((e) & 1) + 1]}
#pragma unroll
            for (int e = 0; e < 4; ++e) T[e] = SW2(2 * j, e) * nl2e;
#pragma unroll
            for (int e = 0; e < 4; ++e) { T[e][0] = __builtin_amdgcn_exp2f(T[e][0]); T[e][1] = __builtin_amdgcn_exp2f(T[e][1]); }
#pragma unroll
            for (int e = 0; e < 4; ++e) { P[e] = SW2(2 * j, e) * SW2(2 * j + 1, e); T[e] = T[e] + 1.f; }
            SBAR();
#pragma unroll
            for (int e = 0; e < 4; ++e) { T[e][0] = __builtin_amdgcn_rcpf(T[e][0]); T[e][1] = __builtin_amdgcn_rcpf(T[e][1]); }
#pragma unroll
            for (int e = 0; e < 4; ++e) { P[e] = P[e] * T[e]; pk[e >> 1][2 * (j & 1) + (e & 1)] = pk2bf(P[e][0], P[e][1]); }
            if constexpr (j & 1) {
                gstore16_sc1(vlin, hb + (j >> 1) * 1024, u32x4{pk[0][0], pk[0][1], pk[0][2], pk[0][3]});
                gstore16_sc1(vlin, hb + (j >> 1) * 1024 + 144 * 1024, u32x4{pk[1][0], pk[1][1], pk[1][2], pk[1][3]});
            } else if constexpr (j == 8) {
                auto s0 = __builtin_amdgcn_permlane16_swap(pk[0][0], pk[1][0], false, false);
                auto s1 = __builtin_amdgcn_permlane16_swap(pk[0][1], pk[1][1], false, false);
                gstore16_sc1(hso, hb + (fodd ? -1024 : 4096), u32x4{s0[0], s1[0], s0[1], s1[1]});
            } else { LAUNDER(pk[0][0]); LAUNDER(pk[0][1]); LAUNDER(pk[1][0]); LAUNDER(pk[1][1]); }
            SBAR();
            if constexpr (j == 4) {
                asm volatile("s_waitcnt vmcnt(4)\n\ts_mov_b64 exec, 1\n\tds_add_u32 %0, %1 offset:4\n\ts_mov_b64 exec, -1" :: "v"(lds0 + MI_ARR), "v"(1u) : "memory");
                SBAR();
            }
        SEND
    }
    DBG_STAMP(3); DBG_STAMPT(21, 256);
    const char* hsrc0 = ws + WS_H + ((size_t)(mt2 * 8 + wm * 2 + kh) * 144 + ks * 36 + kh * 18) * 1024;
    const char* hsrc1 = ws + WS_H + ((size_t)(mt2 * 8 + wm * 2 + 1 - kh) * 144 + ks * 36 + kh * 18) * 1024;
    bf16x8 hf[4][4];
    if (!X_H) { for (int i = 0; i < 4; ++i) { hf[3][i] = xb[0][0]; LAUNDER(hf[3][i]); } }
#define G2_H(s, i) gload16_sc1(hf[(s) & 3][i], vlin, (((i) & 1) ? hsrc1 : hsrc0) + ((s) * 2 + ((i) >> 1)) * 1024)
#define H_PUBLISH() do { asm volatile("s_waitcnt vmcnt(0)" ::: "memory"); unsigned n_ = 0; \
        if (lane == 0) n_ = __hip_atomic_fetch_add((LDSP unsigned*)(uintptr_t)(lds0 + MI_ARR), 1u, __ATOMIC_RELAXED, __HIP_MEMORY_SCOPE_WORKGROUP); \
        if (__builtin_amdgcn_readfirstlane(n_) == 7u) gstore4_sc1_m(SY_HF + (mt1 * 32 + ft) * 128, sync, 1u, 1ull); } while (0)
    const float dD = (cch & 1) ? bfhi(sdw) : bflo(sdw);
    u16* yrow = (u16*)(ws + WS_Y2T) + (size_t)cch * S_;
    SBAR();
#define LDS_WAIT8(off) do { for (unsigned sp_ = 0; sp_ < (1u << 22) && __builtin_amdgcn_readfirstlane(ldsread4(lds0 + MI_ARR + (off))) < 8u; ++sp_) __builtin_amdgcn_s_sleep(1); asm volatile("" ::: "memory"); } while (0)
    {
        f32x4 c00 = {}, c01 = {}, c10 = {}, c11 = {};
        s16x4 El[8], Eh[8]; bf16x8 b1[8], b0[8];
#define CV_BO(F) (((F) >> 1) * 160 + ((F) & 1) * 64)
#define CV_E(F) CAT8(El[(F) & 7], Eh[(F) & 7])
#define CQ_LD(F) do { El[(F) & 7] = ldsread8v(cvA + 64 * (F)); Eh[(F) & 7] = ldsread8v(cvA + 64 * (F) + 8); b1[(F) & 7] = ldsread16(cvB + CV_BO(F)); } while (0)
#define LDS_FLAG(off, v) asm volatile("s_mov_b64 exec, 1\n\tds_add_u32 %0, %1\n\ts_mov_b64 exec, -1" :: "v"(lds0 + MI_ARR + (off)), "v"(v) : "memory")
        const int p = wm & 1;
        const unsigned red = lds0 + CV_RED + wm * 2048 + vlin;
        LDS_WAIT8(4);
        DBG_STAMP(10);
        if (wid < 4) {
#pragma unroll
            for (int F = 0; F < 3; ++F) { CQ_LD(F); b0[F] = ldsread16(cvB + 16 * 160 + CV_BO(F)); }
            SBAR();
#pragma unroll
            for (int F = 0; F < 32; ++F) {
                const int G = F + 3;
                if (F > 0) c01 = MFMA16(CV_E(F - 1), b1[F & 7], c01);
                if (G < 32) El[G & 7] = ldsread8v(cvA + 64 * G);
                SBAR();
                if (F > 0) c00 = MFMA16(CV_E(F - 1), b0[F & 7], c00);
                if (G < 32) Eh[G & 7] = ldsread8v(cvA + 64 * G + 8);
                SBAR();
                c11 = MFMA16(CV_E(F), b1[F & 7], c11);
                if (G < 32) b1[G & 7] = ldsread16(cvB + CV_BO(G));
                SBAR();
                c10 = MFMA16(CV_E(F), b0[F & 7], c10);
                if (G < 32) b0[G & 7] = ldsread16(cvB + 16 * 160 + CV_BO(G));
                SBAR();
                if (F == 8) { H_PUBLISH(); SBAR(); }
                if (F >= 9 && F < 21) { G2_W((F - 9) / 6, (F - 9) % 6); SBAR(); }
            }
            conv_out(c00, ccb + CV_U, p, 0, lane, dD, yrow); conv_out(c10, ccb + CV_U, p + 2, 0, lane, dD, yrow);
            LDS_WAIT8(16 + wm * 4);
            c01 += *(const LDSP f32x4*)(uintptr_t)red; c11 += *(const LDSP f32x4*)(uintptr_t)(red + 1024);
            conv_out(c01, ccb + CV_U, p, 1, lane, dD, yrow); conv_out(c11, ccb + CV_U, p + 2, 1, lane, dD, yrow);
        } else {
            El[7] = ldsread8v(cvA + 64 * 31); Eh[7] = ldsread8v(cvA + 64 * 31 + 8); CQ_LD(32); CQ_LD(33);
#pragma unroll
            for (int G = 34; G < 38; ++G) { El[G & 7] = ldsread8v(cvA + 64 * G); Eh[G & 7] = ldsread8v(cvA + 64 * G + 8); }
            SBAR();
#pragma unroll
            for (int F = 32; F < 64; ++F) {
                const int G = F + 6;
                c01 = MFMA16(CV_E(F - 1), b1[F & 7], c01);
                if (G < 64) { El[G & 7] = ldsread8v(cvA + 64 * G); Eh[G & 7] = ldsread8v(cvA + 64 * G + 8); }
                SBAR();
                c11 = MFMA16(CV_E(F), b1[F & 7], c11);
                if (F + 2 < 64) { b1[(F + 2) & 7] = shr1z(b1[F & 7]); LAUNDER(b1[(F + 2) & 7]); }
                SBAR();
                if (F == 38) { H_PUBLISH(); SBAR(); DBG_STAMPT(11, 256); }
                if (F >= 40 && (F & 1) == 0) { G2_W((F - 40) / 12, ((F - 40) >> 1) % 6); SBAR(); }
            }
            *(LDSP f32x4*)(uintptr_t)red = c01; *(LDSP f32x4*)(uintptr_t)(red + 1024) = c11;
            LDS_FLAG(16 + wm * 4, 8u);
            if (wm < 3) {
                const int dq = (2 - cci) * CV_CH;
                cvA += dq + 32 * ks - 64 - 32 * p + (wm == 1 ? 2048 : 0); cvB += dq + (wm >= 1 ? 16 * 160 : 0);
                CQ_LD(0); CQ_LD(1); CQ_LD(2); CQ_LD(3); CQ_LD(4);
                if (wm != 1 && ks < 2) { El[0] = s16x4{}; Eh[0] = s16x4{}; }
                c01 = f32x4{};
                SBAR();
#pragma unroll
                for (int F = 0; F < 32; ++F) {
                    if (F & 1) c01 = MFMA16(CV_E(F), b1[F & 7], c01); else c00 = MFMA16(CV_E(F), b1[F & 7], c00);
                    if (F + 5 < 32) CQ_LD(F + 5);
                    SBAR();
                }
                c00 += c01;
                const unsigned rq = lds0 + CV_RED + 8192 + vlin;
                if (wm == 1) { *(LDSP f32x4*)(uintptr_t)rq = c00; LDS_FLAG(8, 8u); }
                if (wm == 0) { LDS_WAIT8(8); c00 += *(const LDSP f32x4*)(uintptr_t)rq; }
                if (wm != 1) conv_out(c00, lds0 + CV_BASE + 2 * CV_CH + CV_U, ks, wm == 0, lane, (ch8 & 1) ? bfhi(sdw8) : bflo(sdw8), (u16*)(ws + WS_Y2T) + (size_t)ch8 * S_);
            } else spin_wait(sync, SY_HF + (mt1 * 32 + ks * 8 + (lane & 7)) * 128, 1u, 0u, 1u, vlin);
            DBG_STAMPT(12, 256);
        }
        SBAR();
        DBG_STAMPT(13, 448); DBG_STAMP(14);
        asm volatile("s_waitcnt vmcnt(6) lgkmcnt(0)\n\ts_barrier" ::: "memory");
        SBAR();
#pragma unroll
        for (int i = 0; i < 4; ++i) G2_H(0, i);
#pragma unroll
        for (int i = 0; i < 4; ++i) G2_H(1, i);
        SBAR();
    }
    DBG_STAMP(4);
    unsigned rW0 = lds0 + (lane >> 5) * G1_BLK + (4 * ((lane >> 4) & 1) + ((lane & 15) >> 2)) * 288 + (lane & 3) * 8;
    unsigned rWS = lds0 + 2 * G2_STG + (kh * 8 + (lane >> 4)) * G1_BLK + ((lane & 15) >> 2) * 288 + (lane & 3) * 8;
    unsigned rWs[2] = {rW0 + kh * 8 * G1_BLK, rW0 + kh * 8 * G1_BLK + 2 * G2_STG};
    LAUNDER(rW0); LAUNDER(rWS); LAUNDER(rWs[0]); LAUNDER(rWs[1]);
#define G2_KK(s, i) ((s) == 7 ? (i) & 1 : (i) / 9)
#define G2_A(s, i) ((s) == 7 ? (i) >> 1 : (i) % 9)
#define G2_SGL(s, i) ((2 * (s) + G2_KK(s, i)) % 9 == 4)
#define G2_AH(s, i, t) (G2_SGL(s, i) ? trread(rWS + G2_KK(s, i) * (4 * G1_BLK) + G2_A(s, i) * 32 + (t) * 4 * 288) \
                                     : trread(rWs[((s) >> 1) & 1] + ((s) & 1) * G2_STG + G2_KK(s, i) * (4 * G1_BLK) + G2_A(s, i) * 32 + (t) * 2 * G1_BLK))
#define G2_AH8(i, t) trread(rW0 + ((i) & 3) * (4 * G1_BLK) + ((i) >> 2) * 32 + (t) * 2 * G1_BLK)
#define G2_NXT(s, i, t) ((i) + 4 < 18 ? G2_AH(s, (i) + 4, t) : ((s) < 7 ? G2_AH((s) + 1, (i) + 4 - 18, t) : G2_AH8((i) + 4 - 18, t)))
#define G2_H8(i) gload16_sc1(hf[0][i], vlin, hsrc0 - kh * (18 * 1024) + (((i) >> 1) * 18 + 16 + ((i) & 1)) * 1024)
#define G2_WAIT(N, s) asm volatile("s_waitcnt vmcnt(" #N ")\n\ts_barrier" : "+v"(hf[(s) & 3][0]), "+v"(hf[(s) & 3][1]), "+v"(hf[(s) & 3][2]), "+v"(hf[(s) & 3][3]) :: "memory")
    f32x4 ac2[9][2];
    s16x4 bl[6], bh[6];
    unsigned yv[9], q8; u32x2 qf[2];
    const int fm = wid & 1, fpc = wid >> 1, row0 = mt2 * 128 + ks * 32;
    const char* yb = ws + WS_Y2T + (size_t)(nt * 144) * (S_ * 2) + (row0 + 16 * fm) * 2;
    const unsigned yo = (lane >> 4) * (S_ * 2) + (lane & 15) * 2;
    const unsigned xo = lds0 + FN_X + wid * 1024 + vlin, xi = lds0 + FN_X + (wid ^ 4) * 1024 + vlin;
    SBAR();
#pragma unroll
    for (int i = 0; i < 4; ++i) { bl[i] = G2_AH(0, i, 0); bh[i] = G2_AH(0, i, 1); }
    SBAR();
    asm volatile("s_waitcnt vmcnt(4)" : "+v"(hf[0][0]), "+v"(hf[0][1]), "+v"(hf[0][2]), "+v"(hf[0][3]) :: "memory");
    SBAR();
    DBG_STAMP(16);
    SFOR(s, 0, 8)
        SFOR(i, 0, 18)
            if constexpr (i == 14 && s < 7) {
                if constexpr (s == 6) G2_WAIT(13, s + 1); else G2_WAIT(10, s + 1);
                SBAR();
                if (wid == 0 && (s == 1 || s == 3 || s == 5)) {
                    if (s == 1) cnt_add(sync, CN_CONV + nt);
                    if (s == 3) glds_poll(0, sync + SY_CNT + (CN_CONV + nt) * 256, lds0 + MI_POLL);
                    if (s == 5) { if (__builtin_amdgcn_readfirstlane(ldsread4(lds0 + MI_POLL)) < 64u) { DBG_STAMP(20); spin_wait(sync, SY_CNT + (CN_CONV + nt) * 256, 64u, GE, 1u, vlin); } }
                    SBAR();
                }
            }
            if constexpr (i == 8 && s == 7) { G2_WAIT(9, 8); SBAR(); }
            {
                const bf16x8 A = CAT8(bl[i % 6], bh[i % 6]);
                if constexpr (s == 0 && i < 9) MFMA16Z(ac2[i][0], A, hf[0][0]); else MFMA16A(ac2[G2_A(s, i)][0], A, hf[s & 3][G2_KK(s, i) * 2]);
                SBAR();
                bl[(i + 4) % 6] = G2_NXT(s, i, 0);
                SBAR();
                if constexpr (s == 0 && i < 9) MFMA16Z(ac2[i][1], A, hf[0][1]); else MFMA16A(ac2[G2_A(s, i)][1], A, hf[s & 3][G2_KK(s, i) * 2 + 1]);
                SBAR();
                bh[(i + 4) % 6] = G2_NXT(s, i, 1);
            }
            if constexpr (s == 7 && (i & 1) == 1 && i >= 3) *(LDSP f32x4*)(uintptr_t)(xo + ((i >> 1) - 1) * 8192) = ac2[(i >> 1) - 1][1];
            SBAR();
            if constexpr (X_H && i >= 14 && s + 3 < 8) G2_H(s + 3, i - 14);
            if constexpr (X_H && i >= 14 && s == 5) G2_H8(i - 14);
            if constexpr (X_W && i < 6 && s + 2 < 9) G2_W(s + 2, i);
            if constexpr (s == 0 && i >= 6 && i < 10) G2_H(2, i - 6);
            if constexpr ((s == 6 && i >= 14) || (s == 7 && i < 5)) {
                constexpr int e = s == 6 ? i - 14 : i + 4;
                if constexpr (e < 8) gload2_sc1(yv[e], yo * 4 - (lane & 15) * 6, yb + (size_t)(fpc * 32 + (e >> 2) * 16 + (e & 3)) * (S_ * 2));
                else gload2_sc1(yv[8], yo, yb + (size_t)(128 + 4 * fpc) * (S_ * 2));
            }
            if constexpr (s == 6 && (i == 7 || i == 9 || i == 11)) {
                const char* qb = (const char*)q + (size_t)(row0 + 16 * fm) * (D_ * 2) + nt * 288;
                if constexpr (i < 11) gload8_a(qf[(i - 7) >> 1], (lane & 15) * (D_ * 2) + (lane >> 4) * 8, qb + (2 * fpc + ((i - 7) >> 1)) * 32);
                else gload2_sc1(q8, (lane & 15) * (D_ * 2) + (lane >> 4) * 2, qb + 256 + fpc * 8);
            }
            SBAR();
        SEND
    SEND
    asm volatile("s_nop 7" : "+v"(ac2[8][1]));
    *(LDSP f32x4*)(uintptr_t)(xo + 8 * 8192) = ac2[8][1];
    SBAR();
    asm volatile("s_waitcnt lgkmcnt(0)\n\ts_barrier" ::: "memory");
    SBAR();
    DBG_STAMP(5);
    float gg, g1, ga[9];
    {
        char* dst = ws + WS_INBOX + (size_t)((idx << 3) | (wm << 1) | (grp & 1)) * INBOX_B;
        const int src = wm == ks ? 0 : ks - (ks > wm ? 1 : 0);
        unsigned long long mown, msend;
        asm("s_cmp_eq_u32 %2, %3\n\ts_cselect_b64 %0, -1, 0\n\ts_cselect_b64 %1, 0, -1" : "=s"(mown), "=s"(msend) : "s"(wm), "s"(ks) : "scc");
        f32x4 c[2]; unsigned pk[4];
        SFOR(i, 0, 40)
            constexpr int a = i >> 2;
            if constexpr (i < 36) MFMA16A(ac2[a][0], CAT8(bl[i % 6], bh[i % 6]), hf[0][i & 3]);
            if constexpr (i == 36) asm volatile("s_nop 7" : "+v"(ac2[8][0]));
            SBAR();
            if constexpr (i + 4 < 36) { bl[(i + 4) % 6] = G2_AH8(i + 4, 0); bh[(i + 4) % 6] = G2_AH8(i + 4, 1); }
            if constexpr ((i & 3) == 0 && a < 9) c[a & 1] = *(const LDSP f32x4*)(uintptr_t)(xi + a * 8192);
            SBAR();
            if constexpr (i == 9) {
                asm volatile("s_waitcnt vmcnt(0)" : "+v"(yv[0]), "+v"(yv[1]), "+v"(yv[2]), "+v"(yv[3]), "+v"(yv[4]), "+v"(yv[5]), "+v"(yv[6]), "+v"(yv[7]), "+v"(yv[8]), "+v"(qf[0]), "+v"(qf[1]), "+v"(q8) :: "memory");
                const float sg = rbf(*(const LDSP float*)(uintptr_t)(lds0 + MI_G + (16 * fm + (lane & 15)) * 4));
                gg = rbf(__builtin_amdgcn_rcpf(1.f + __builtin_amdgcn_exp2f(sg * -1.4426950408889634f)));
                g1 = rbf(1.f - gg); LAUNDER(gg); LAUNDER(g1);
                SBAR();
            }
            if constexpr (i >= 11 && i < 29 && (i & 1) == 1) {
                constexpr int e = (i - 11) >> 1;
                const float qx = e == 8 ? bflo(q8) : ((e & 1) ? bfhi(qf[(e >> 2) & 1][(e >> 1) & 1]) : bflo(qf[(e >> 2) & 1][(e >> 1) & 1]));
                ga[e] = rbf(gg * rbf(qx * bflo(yv[e]))); LAUNDER(ga[e]);
                SBAR();
            }
            if constexpr ((i & 3) == 2 && a >= 1) {
                constexpr int b = a - 1;
                f32x4 sum = ac2[b][0] + c[b & 1];
                asm volatile("s_mov_b64 exec, %2\n\tds_write_b128 %0, %1 offset:%3\n\ts_mov_b64 exec, -1" :: "v"(b < 8 ? xi : xi + 8192), "v"(sum), "s"(mown), "i"((b < 8 ? b : 7) * 8192) : "memory");
                pk[2 * (b & 1)] = nosent(pk2bf(sum[0], sum[1])); pk[2 * (b & 1) + 1] = nosent(pk2bf(sum[2], sum[3]));
                if constexpr (b == 8) asm volatile("s_mov_b64 exec, %3\n\tglobal_store_dwordx2 %0, %1, %2 sc1\n\ts_mov_b64 exec, -1"
                                         :: "v"(lane * 8), "v"(u32x2{pk[0], pk[1]}), "s"(dst + 24576 + (kh * 3 + src) * 512), "s"(msend) : "memory");
                else if constexpr (b & 1) asm volatile("s_mov_b64 exec, %3\n\tglobal_store_dwordx4 %0, %1, %2 sc1\n\ts_mov_b64 exec, -1\n\ts_nop 0"
                                             :: "v"(vlin), "v"(u32x4{pk[0], pk[1], pk[2], pk[3]}), "s"(dst + ((kh * 4 + (b >> 1)) * 3 + src) * 1024), "s"(msend) : "memory");
                SBAR();
            }
        SEND
    }
    DBG_STAMP(17);
    {
        const int tl = 16 * fm + (lane & 15), g4 = lane >> 4;
        const char* ib = inbox + (fm * 4 + fpc) * 3072;
        const char* ib8 = inbox + 24576 + fm * 1536;
        const unsigned o8 = (fpc * 16 + (lane & 15)) * 8 + (g4 >> 1) * 4;
        u32x4 in[3]; unsigned i8[3];
        unsigned first = 1;
#pragma nounroll
        for (unsigned spins = 0;; ++spins) {
            asm volatile("global_load_dwordx4 %0, %6, %7 sc1\n\tglobal_load_dwordx4 %1, %6, %7 offset:1024 sc1\n\tglobal_load_dwordx4 %2, %6, %7 offset:2048 sc1\n\t"
                         "global_load_dword %3, %8, %9 sc1\n\tglobal_load_dword %4, %8, %9 offset:512 sc1\n\tglobal_load_dword %5, %8, %9 offset:1024 sc1\n\t"
                         "s_cmp_eq_u32 %10, 0\n\ts_cbranch_scc1 1f\n\ts_waitcnt lgkmcnt(0)\n\ts_barrier\n1:\n\ts_waitcnt vmcnt(0)"
                         : "=&v"(in[0]), "=&v"(in[1]), "=&v"(in[2]), "=&v"(i8[0]), "=&v"(i8[1]), "=&v"(i8[2]) : "v"(vlin), "s"(ib), "v"(o8), "s"(ib8), "s"(first) : "memory", "scc");
            first = 0;
            unsigned mx = i8[0] > i8[1] ? i8[0] : i8[1]; mx = mx > i8[2] ? mx : i8[2];
#pragma unroll
            for (int j = 0; j < 3; ++j)
#pragma unroll
                for (int e = 0; e < 4; ++e) mx = mx > in[j][e] ? mx : in[j][e];
            if (wave_all(mx != 0xffffffffu) || spins >= SPIN_CAP) break;
        }
        DBG_STAMP(6);
        float own[9];
        const unsigned pb = lds0 + FN_X + (((1 - fm) * 4 + ks) * 1024);
#pragma unroll
        for (int h = 0; h < 2; ++h) {
            const f32x4 p0 = *(const LDSP f32x4*)(uintptr_t)(pb + (2 * fpc + h) * 8192 + vlin);
#pragma unroll
            for (int e = 0; e < 4; ++e) own[4 * h + e] = p0[e];
        }
        own[8] = *(const LDSP float*)(uintptr_t)(pb + 8 * 8192 + (fpc * 16 + (lane & 15)) * 16 + g4 * 4);
        float o[9];
#pragma unroll
        for (int e = 0; e < 9; ++e) {
            float b = 0.f;
#pragma unroll
            for (int j = 0; j < 3; ++j) {
                if (e < 8) b += (e & 1) ? bfhi(in[j][e >> 1]) : bflo(in[j][e >> 1]);
                else b += (g4 & 1) ? bfhi(i8[j]) : bflo(i8[j]);
            }
            o[e] = ga[e] + rbf(g1 * rbf(b + own[e]));
        }
        u16* orow = out + (size_t)(row0 + tl) * D_ + nt * 144;
        *(u32x2*)(orow + fpc * 32 + g4 * 4) = u32x2{pk2bf(o[0], o[1]), pk2bf(o[2], o[3])};
        *(u32x2*)(orow + fpc * 32 + 16 + g4 * 4) = u32x2{pk2bf(o[4], o[5]), pk2bf(o[6], o[7])};
        orow[128 + fpc * 4 + g4] = (u16)pk2bf(o[8], 0.f);
    }
    DBG_STAMP(7);
}

#define DBG_ARG
extern "C" void kernel_launch(void* const* d_in, const int* in_sizes, int n_in, void* d_out, int out_size, void* d_ws, size_t ws_size, hipStream_t stream) {
    (void)hipFuncSetAttribute((const void*)k_mega, hipFuncAttributeMaxDynamicSharedMemorySize, L_TOTAL);
    k_mega<<<256, 512, L_TOTAL, stream>>>((const u16*)d_in[0], (const u16*)d_in[1], (const u16*)d_in[2], (const u16*)d_in[3], (const u16*)d_in[4], (const u16*)d_in[5],
                                          (const u16*)d_in[7], (const u16*)d_in[8], (const u16*)d_in[9], (const u16*)d_in[10], (u16*)d_out, (char*)d_ws DBG_ARG);
}
```

```cpp
#include <hip/hip_runtime.h>
#include <hip/hip_bf16.h>
#include <stdint.h>
typedef unsigned short u16;
typedef __attribute__((ext_vector_type(8))) short bf16x8;
typedef __attribute__((ext_vector_type(4))) short s16x4;
typedef __attribute__((ext_vector_type(4))) float f32x4;
typedef __attribute__((ext_vector_type(2))) float f32x2;
typedef __attribute__((ext_vector_type(16))) float f32x16;
typedef __attribute__((ext_vector_type(4))) unsigned u32x4;
typedef __attribute__((ext_vector_type(2))) unsigned u32x2;
typedef short v4i16_t __attribute__((ext_vector_type(4)));
#define LDSP __attribute__((address_space(3)))
#define DI __device__ __forceinline__
#define SBAR() __builtin_amdgcn_sched_barrier(0)
#define LAUNDER(x) asm volatile("" : "+v"(x))
#define LAUNDER_S(x) asm volatile("" : "+s"(x))

constexpr int S_ = 2048, D_ = 576, F_ = 4608;

DI float asf(unsigned u) { return __builtin_bit_cast(float, u); }
DI unsigned asu(float f) { return __builtin_bit_cast(unsigned, f); }
DI unsigned pk2bf(float lo, float hi) {
    typedef __attribute__((ext_vector_type(2))) __bf16 bf2;
    typedef __attribute__((ext_vector_type(2))) float f2;
    f2 v = {lo, hi};
    bf2 r = __builtin_convertvector(v, bf2);
    return __builtin_bit_cast(unsigned, r);
}
DI float rbf(float f) { return asf(pk2bf(0.f, f)); }
DI float bflo(unsigned w) { return asf(w << 16); }
DI float bfhi(unsigned w) { return asf(w & 0xffff0000u); }
DI bool wave_all(bool p) { return __builtin_amdgcn_ballot_w64(p) == ~0ull; }

#define NT_KV ""
#define GLDS16(voff, sbase, ldsaddr, SC) asm volatile("s_mov_b32 m0, %2\n\ts_nop 0\n\tglobal_load_lds_dwordx4 %0, %1" SC :: "v"(voff), "s"(sbase), "s"(ldsaddr) : "memory")
DI void glds_full(unsigned voff, const void* sbase, unsigned ldsaddr) { GLDS16(voff, sbase, ldsaddr, ""); }
DI void glds_sc1(unsigned voff, const void* sbase, unsigned ldsaddr) { GLDS16(voff, sbase, ldsaddr, " sc1"); }
DI void glds_48(unsigned voff, const void* sbase, unsigned ldsaddr) {
    asm volatile("s_mov_b32 m0, %2\n\ts_mov_b32 exec_hi, 0xffff\n\tglobal_load_lds_dwordx4 %0, %1\n\ts_mov_b32 exec_hi, -1" :: "v"(voff), "s"(sbase), "s"(ldsaddr) : "memory");
}
DI void glds_48b(unsigned voff, const void* sbase, unsigned ldsaddr) {
    asm volatile("s_mov_b32 m0, %2\n\ts_mov_b32 exec_hi, 0xffff\n\tglobal_load_lds_dwordx4 %0, %1\n\ts_mov_b32 exec_hi, -1" :: "v"(voff), "s"(sbase), "s"(ldsaddr) : "memory");
}
DI void glds_m(unsigned voff, const void* sbase, unsigned ldsaddr, unsigned long long mask) {
    asm volatile("s_mov_b32 m0, %2\n\ts_mov_b64 exec, %3\n\tglobal_load_lds_dwordx4 %0, %1\n\ts_mov_b64 exec, -1" :: "v"(voff), "s"(sbase), "s"(ldsaddr), "s"(mask) : "memory");
}
DI void glds_m_nt(unsigned voff, const void* sbase, unsigned ldsaddr, unsigned long long mask) {
    asm volatile("s_mov_b32 m0, %2\n\ts_mov_b64 exec, %3\n\tglobal_load_lds_dwordx4 %0, %1" NT_KV "\n\ts_mov_b64 exec, -1" :: "v"(voff), "s"(sbase), "s"(ldsaddr), "s"(mask) : "memory");
}
DI void glds_poll(unsigned voff, const void* sbase, unsigned ldsaddr) {
    asm volatile("s_mov_b32 m0, %2\n\ts_mov_b64 exec, 0xffff\n\tglobal_load_lds_dword %0, %1 sc1\n\ts_mov_b64 exec, -1" :: "v"(voff), "s"(sbase), "s"(ldsaddr) : "memory");
}
DI void glds_dw(unsigned voff, const void* sbase, unsigned ldsaddr) {
    asm volatile("s_mov_b32 m0, %2\n\ts_nop 0\n\tglobal_load_lds_dword %0, %1" :: "v"(voff), "s"(sbase), "s"(ldsaddr) : "memory");
}
DI void glds_m16(unsigned voff, const void* sbase, unsigned ldsaddr) {
    asm volatile("s_mov_b32 m0, %2\n\ts_mov_b64 exec, 0xffff\n\tglobal_load_lds_dword %0, %1\n\ts_mov_b64 exec, -1" :: "v"(voff), "s"(sbase), "s"(ldsaddr) : "memory");
}
DI void gstore16_sc1(unsigned voff, void* sbase, u32x4 d) { asm volatile("global_store_dwordx4 %0, %1, %2 sc1\n\ts_nop 1" :: "v"(voff), "v"(d), "s"(sbase) : "memory"); }
DI void gstore8_sc1(unsigned voff, void* sbase, u32x2 d) { asm volatile("global_store_dwordx2 %0, %1, %2 sc1" :: "v"(voff), "v"(d), "s"(sbase) : "memory"); }
DI void gstore4_sc1_m(unsigned voff, void* sbase, unsigned d, unsigned long long mask) {
    asm volatile("s_mov_b64 exec, %3\n\tglobal_store_dword %0, %1, %2 sc1\n\ts_mov_b64 exec, -1" :: "v"(voff), "v"(d), "s"(sbase), "s"(mask) : "memory");
}
template <class T> DI void gload16_sc1(T& d, unsigned voff, const void* sbase) { asm volatile("global_load_dwordx4 %0, %1, %2 sc1" : "=v"(d) : "v"(voff), "s"(sbase) : "memory"); }
DI void gload16_a(u32x4& d, unsigned voff, const void* sbase) { asm volatile("global_load_dwordx4 %0, %1, %2" : "=v"(d) : "v"(voff), "s"(sbase) : "memory"); }
DI void gload8_a(u32x2& d, unsigned voff, const void* sbase) { asm volatile("global_load_dwordx2 %0, %1, %2" : "=v"(d) : "v"(voff), "s"(sbase) : "memory"); }
DI void gload2_sc1(unsigned& d, unsigned voff, const void* sbase) { asm volatile("global_load_ushort %0, %1, %2 sc1" : "=v"(d) : "v"(voff), "s"(sbase) : "memory"); }
DI s16x4 trread(unsigned a) { return __builtin_bit_cast(s16x4, __builtin_amdgcn_ds_read_tr16_b64_v4i16((LDSP v4i16_t*)(uintptr_t)a)); }
DI bf16x8 trfrag(unsigned a, int o0, int o1) {
    s16x4 lo = trread(a + o0), hi = trread(a + o1);
    return __builtin_shufflevector(lo, hi, 0, 1, 2, 3, 4, 5, 6, 7);
}
DI bf16x8 ldsread16(unsigned a) { return *(const LDSP bf16x8*)(uintptr_t)a; }
DI s16x4 ldsread8v(unsigned a) { return *(const volatile LDSP s16x4*)(uintptr_t)a; }
DI unsigned ldsread4(unsigned a) { return *(const volatile LDSP unsigned*)(uintptr_t)a; }
#define CAT8(lo, hi) __builtin_shufflevector(lo, hi, 0, 1, 2, 3, 4, 5, 6, 7)
#define MFMA32(a, b, c) __builtin_amdgcn_mfma_f32_32x32x16_bf16(a, b, c, 0, 0, 0)
#define MFMA16(a, b, c) __builtin_amdgcn_mfma_f32_16x16x32_bf16(a, b, c, 0, 0, 0)
#define STR_(x) #x
#define WAITV_BAR(N) asm volatile("s_waitcnt vmcnt(" STR_(N) ")\n\ts_barrier" ::: "memory")
#define MFMA16A(c, a, b) asm volatile("v_mfma_f32_16x16x32_bf16 %0, %1, %2, %0" : "+v"(c) : "v"(a), "v"(b))
#define MFMA16Z(c, a, b) asm volatile("v_mfma_f32_16x16x32_bf16 %0, %1, %2, 0" : "=&v"(c) : "v"(a), "v"(b))
template <int V> struct IC { static constexpr int value = V; };
template <int I, int N, class F> DI void static_for(F&& f) { if constexpr (I < N) { f(IC<I>{}); static_for<I + 1, N>(f); } }
#define SFOR(var, lo, hi) static_for<lo, hi>([&](auto var##_c) __attribute__((always_inline)) { constexpr int var = decltype(var##_c)::value;
#define SEND });
DI bf16x8 shr1z(bf16x8 v) {
    u32x4 a = __builtin_bit_cast(u32x4, v), r;
#pragma unroll
    for (int e = 0; e < 4; ++e) r[e] = __builtin_amdgcn_update_dpp(0, a[e], 0x111, 0xf, 0xf, true);
    return __builtin_bit_cast(bf16x8, r);
}
constexpr size_t WS_H = 0, WS_INBOX = 20u << 20, WS_KVR = 40u << 20, WS_Y2T = 44u << 20, WS_SYNC = 48u << 20;
constexpr int SY_NONCE = 0, SY_ECHO = 1024, SY_CNT = 4096;
constexpr int CN_PREP = 0, CN_CONV = 8, CN_N = 12, SY_HF = 8192;
constexpr int INBOX_B = 27648;
constexpr unsigned SPIN_CAP = 1u << 22;
constexpr unsigned CODE_BYTES = 34000;
#define LOOPN 10
constexpr int D0 = 13, D1 = 39;
constexpr int G1_BLK = 2432, G1_WMAT = 9616, G1_WSLOT = 19216, G1_XOFF = 96128, L_MISC = G1_XOFF + 65536, L_TOTAL = 163840;
constexpr int MI_GW = L_MISC, MI_POLL = L_MISC + 1152, MI_ARR = L_MISC + 1216, MI_G = L_MISC + 1280, MI_DUMMY = L_MISC + 1536;
constexpr int CV_HC = 4160, CV_U = 4 * CV_HC, CV_CH = CV_U + 47 * 160 + 32, CV_BASE = L_MISC - 3 * CV_CH - 9216, CV_RED = CV_BASE + 3 * CV_CH;
constexpr int G2_STG = 16 * G1_BLK;
static_assert(CV_BASE >= 2 * G2_STG && CV_BASE >= 3 * G1_WSLOT && CV_BASE % 64 == 0 && 4 * G2_STG <= L_MISC, "lds map");
constexpr int FN_X = G2_STG;

DI void cnt_add(char* sync, int c) {
    unsigned one = 1, off = SY_CNT + c * 256;
    asm volatile("s_mov_b64 exec, 1\n\tglobal_atomic_add %0, %1, %2 sc1\n\ts_mov_b64 exec, -1" :: "v"(off), "v"(one), "s"(sync) : "memory");
}
DI void spin_wait(char* sync, unsigned voff, unsigned lo, unsigned span, unsigned noecho, unsigned vlin) {
    unsigned cnt, t; u32x4 e;
    asm volatile(
        "s_mov_b32 %0, 0x400000\n"
        "1:\n\t"
        "s_cmp_lg_u32 %8, 0\n\t"
        "s_cbranch_scc1 2f\n\t"
        "global_load_dwordx4 %2, %7, %4 sc1\n\t"
        "s_waitcnt vmcnt(0)\n\t"
        "global_store_dwordx4 %7, %2, %4 offset:1024 sc1\n"
        "2:\n\t"
        "global_load_dword %1, %3, %4 sc1\n\t"
        "s_waitcnt vmcnt(0)\n\t"
        "v_subrev_u32 %1, %5, %1\n\t"
        "v_cmp_ge_u32 vcc, %6, %1\n\t"
        "s_cmp_eq_u64 vcc, -1\n\t"
        "s_cbranch_scc1 3f\n\t"
        "s_sleep 4\n\t"
        "s_sub_u32 %0, %0, 1\n\t"
        "s_cbranch_scc0 1b\n"
        "3:"
        : "=&s"(cnt), "=&v"(t), "=&v"(e) : "v"(voff), "s"(sync), "s"(lo), "s"(span), "v"(vlin), "s"(noecho) : "memory", "vcc", "scc");
}
constexpr unsigned GE = 0x7fffffffu;
DI u32x2 conv_kv(unsigned ubase, int m, int n, int lane) { return *(const LDSP u32x2*)(uintptr_t)(ubase + (31 - 16 * n - (lane & 15)) * 160 + (60 - 16 * m - 4 * (lane >> 4)) * 2); }
DI void conv_out(f32x4 acc, u32x2 uu, int m, int n, int lane, float dD, u16* yrow) {
    float kv[4] = {bfhi(uu[1]), bflo(uu[1]), bfhi(uu[0]), bflo(uu[0])};
    float o[4];
#pragma unroll
    for (int r = 0; r < 4; ++r) o[r] = rbf(acc[r]) + rbf(kv[r] * dD);
    u32x2 w = {pk2bf(o[0], o[1]), pk2bf(o[2], o[3])};
    gstore8_sc1((lane & 15) * 32 + (lane >> 4) * 8, yrow + (4 * n + m) * 256, w);
}
DI unsigned nosent(unsigned x) { return x < 0xfffffffeu ? x : 0xfffffffeu; }

__global__ __launch_bounds__(512, 2) void k_mega(const u16* __restrict__ q, const u16* __restrict__ kk_, const u16* __restrict__ vv_, const u16* __restrict__ sk,
                                                      const u16* __restrict__ hk, const u16* __restrict__ sD, const u16* __restrict__ w1, const u16* __restrict__ w3,
                                                      const u16* __restrict__ w2, const u16* __restrict__ gw, u16* __restrict__ out, char* __restrict__ ws) {
    extern __shared__ __attribute__((aligned(16))) char smem[];
    const char* pc0 = (const char*)__builtin_amdgcn_s_getpc();
    int tid = __builtin_amdgcn_workitem_id_x(), lane = tid & 63;
    {
        if (tid < 64) glds_dw(tid * 128, pc0, (unsigned)(uintptr_t)smem + MI_DUMMY);
    }
    SBAR();
    asm volatile("" :: "s"(q), "s"(kk_), "s"(vv_), "s"(sk), "s"(hk), "s"(sD), "s"(w1), "s"(w3), "s"(w2), "s"(gw), "s"(out), "s"(ws));
    const int wid = __builtin_amdgcn_readfirstlane(tid >> 6);
    const int wm = wid & 3, wf = wid >> 2, kh = wf;
    const int bid = __builtin_amdgcn_workgroup_id_x(), grp = bid & 7, idx = bid >> 3;
    const unsigned lds0 = (unsigned)(uintptr_t)smem;
    char* sync = ws + WS_SYNC;
    const int mt1 = idx & 7, ft = grp * 4 + (idx >> 3);
    const int mt2 = mt1 * 2 + (grp & 1), nt = idx >> 3, ks = grp >> 1;
    const int ptt = grp * 4 + (idx >> 3), pdt = idx & 7;
    const int quad = nt * 16 + mt2, ch0 = quad * 9 + 2 * ks, ch8 = quad * 9 + 8;
    const unsigned vlin = lane * 16;

    const int fodd = ft & 1;
    unsigned voffW[3];
#pragma unroll
    for (int i = 0; i < 3; ++i) {
        const int c = i * 48 + (lane < 48 ? lane : 0), r = c / 18, c0 = c - r * 18; int cc = c0 + 2 * fodd; cc = cc >= 18 ? cc - 18 : cc;
        voffW[i] = r * (F_ * 2) + cc * 16;
    }
    const char* wsrc = (const char*)(wf ? w3 : w1) + ft * 288 + (size_t)wm * (8 * F_ * 2);
    const unsigned wdst = lds0 + wf * G1_WMAT + wm * G1_BLK;
    unsigned voffX[2];
#pragma unroll
    for (int i = 0; i < 2; ++i) { int r = 8 * i + (lane >> 3); voffX[i] = r * (D_ * 2) + (((lane & 7) ^ ((r >> 1) & 7)) * 16); }
    const char* xsrc = (const char*)q + (size_t)(mt1 * 256 + wid * 32) * (D_ * 2);
    const unsigned xdst = lds0 + G1_XOFF + wid * 4096;
#define G1_W(u, slotoff, i) glds_48(voffW[i], wsrc + (size_t)(u) * (32 * F_ * 2), wdst + (slotoff) + (i) * 768)
#define G1_X(s, i) glds_full(voffX[(i) & 1], xsrc + (s) * 128 + ((i) >> 1) * (16 * D_ * 2), xdst + ((s) & 1) * 32768 + (i) * 1024)
    if (wid == 7) { glds_full(vlin, gw, lds0 + MI_GW); glds_m(vlin, (const char*)gw + 1024, lds0 + MI_GW + 1024, 0xffull); }
    G1_X(0, 0); G1_X(0, 1); G1_X(0, 2); G1_X(0, 3);
    G1_W(0, 0, 0); G1_W(0, 0, 1); G1_W(0, 0, 2);
    u32x2 skv[2];
    {
        const int t0 = ptt * 64;
#pragma unroll
        for (int j = 0; j < 3; ++j) {
            const int p = wid + 8 * j;
            if (p < 19) {
                const bool isk = p >= 9; const int c = (isk ? p - 9 : p) * 64 + lane;
                int row = c / 9; const int cc = c - row * 9; row += t0 - (isk ? 3 : 0); row = row < 0 ? 0 : row;
                glds_m_nt(row * (D_ * 2) + cc * 16, (const char*)(isk ? kk_ : vv_) + pdt * 144, lds0 + 4 * G1_WSLOT + p * 1024, p == 18 ? ((1ull << 27) - 1) : ~0ull);
            }
        }
        gload8_a(skv[0], (lane & 7) * 8, (const char*)sk + pdt * 576 + wid * 64);
        gload8_a(skv[1], 0u, (const char*)sk + pdt * 576 + 512 + wid * 8);
        if (wid == 0) {
            glds_poll(bid * 4, sync + SY_ECHO, lds0 + MI_POLL);
            if (bid == 0) gstore4_sc1_m(SY_CNT + lane * 256, sync, 0u, (1ull << CN_N) - 1);
        }
        if (bid == 0 && wid >= 4) gstore4_sc1_m(SY_HF + (tid - 256) * 128, sync, 0u, ~0ull);
        if (tid >= 64 && tid < 68) *(LDSP u32x4*)(uintptr_t)(lds0 + MI_ARR + (tid & 3) * 16) = u32x4{0u, 0u, 0u, 0u};
    }
    G1_W(1, G1_WSLOT, 0); G1_W(1, G1_WSLOT, 1); G1_W(1, G1_WSLOT, 2);
    if (wid == 1 + (idx & 3)) { unsigned o = tid * 128; o = o < CODE_BYTES ? o : 0; glds_dw(o, pc0, lds0 + MI_DUMMY); }
    const unsigned rW = lds0 + (lane >> 4) * G1_BLK + ((lane & 15) >> 2) * 288 + wf * 128 + (lane & 3) * 8;
    const unsigned rWd = ((lane >> 1) & 1) * (G1_WMAT - 16) + 256 - wf * 112;
    const unsigned rX = lds0 + G1_XOFF + (wm * 64 + (lane & 15)) * 128 + (((lane >> 4) ^ ((lane >> 1) & 7)) * 16);
#define G1_AH(base, a, t) ((a) == 8 ? trread((base) + rWd + (t) * 4 * 288) : trread((base) + ((a) & 1) * G1_WMAT + ((a) >> 1) * 32 + (t) * 4 * 288))
#define G1_XF(u, m) ldsread16((rX ^ (((u) & 1) * 64)) + (((u) >> 1) & 1) * 32768 + (m) * 2048)
    f32x4 acc[18][2];
    s16x4 al[3], ah[3]; bf16x8 xb[2][4];
    const int cci = wm >> 1, cch = ch0 + cci;
    const unsigned ccb = lds0 + CV_BASE + cci * CV_CH;
    unsigned sdw, sdw8;
    asm volatile("s_load_dword %0, %2, 0x0\n\ts_load_dword %1, %3, 0x0\n\ts_waitcnt lgkmcnt(0)" : "=&s"(sdw), "=&s"(sdw8) : "s"((const char*)sD + (cch >> 1) * 4), "s"((const char*)sD + (ch8 >> 1) * 4) : "memory");
    SBAR();
    asm volatile("s_waitcnt vmcnt(7)\n\ts_barrier" ::: "memory");
    SBAR();
    unsigned nonce = 0;
    u32x2 kr[3], vr[2]; unsigned ke[5], pw[5]; float kf[16], sc[8], po[9], px, py;
    const int poct = 2 * (lane >> 4) + ((lane >> 3) & 1);
    char* kvb = ws + WS_KVR + (size_t)(pdt * 72) * (S_ * 2) + (S_ - 64 - ptt * 64) * 2;
    unsigned pa = lds0 + 4 * G1_WSLOT + (8 * (2 * (lane >> 4) + ((lane & 3) >> 1)) + ((lane & 15) >> 2)) * 144 + (lane & 1) * 8 + wid * 16;
    unsigned pe = lds0 + 4 * G1_WSLOT + lane * 144 + 128 + wid * 2;
    unsigned pso = (wid * 8 + (lane & 7)) * (S_ * 2) + (7 - poct) * 16;
    unsigned pm0 = (ptt == 0 && poct == 0) ? 0u : ~0u;
#define PREP_GAP(g_) do { const int g = (g_) - 12; \
        if (g == -1) { asm volatile("s_waitcnt vmcnt(8)\n\ts_barrier" : "+v"(skv[0]), "+v"(skv[1]) :: "memory");        \
            if (wid == 0) { nonce = __builtin_amdgcn_readfirstlane(ldsread4(lds0 + MI_POLL)) + 1u; gstore4_sc1_m(SY_NONCE + bid * 4, sync, nonce, 1ull); } } \
        if (g == 0) { kr[0] = __builtin_bit_cast(u32x2, trread(pa + 9216)); kr[1] = __builtin_bit_cast(u32x2, trread(pa + 9216 + 576)); } \
        if (g == 1) { kr[2] = __builtin_bit_cast(u32x2, trread(pa + 9216 + 1152)); vr[0] = __builtin_bit_cast(u32x2, trread(pa)); } \
        if (g == 2) { vr[1] = __builtin_bit_cast(u32x2, trread(pa + 576)); ke[4] = *(const LDSP u16*)(uintptr_t)pe; } \
        if (g == 3 || g == 4) { ke[2 * ((g - 3) & 1)] = *(const LDSP u16*)(uintptr_t)(pe + 9216 + 2 * (g - 3) * 144); ke[2 * ((g - 3) & 1) + 1] = *(const LDSP u16*)(uintptr_t)(pe + 9216 + (2 * (g - 3) + 1) * 144); } \
        if (g >= 0 && g < 4) { const unsigned w_ = skv[(g >> 1) & 1][g & 1]; sc[2 * (g & 3)] = bflo(w_); sc[2 * (g & 3) + 1] = bfhi(w_); LAUNDER(sc[2 * (g & 3)]); LAUNDER(sc[2 * (g & 3) + 1]); } \
        if (g == 6) { kr[0][0] &= pm0; kr[0][1] &= pm0 | 0xffff0000u; LAUNDER(kr[0]); } \
        if (g >= 7 && g < 11) { _Pragma("unroll") for (int e_ = 0; e_ < 3; ++e_) { const int n_ = ((g - 7) & 3) * 3 + e_; const unsigned w_ = kr[n_ >> 2][(n_ >> 1) & 1]; \
                kf[n_] = (n_ & 1) ? bfhi(w_) : bflo(w_); LAUNDER(kf[n_]); } } \
        if (g == 11 || g == 12) { _Pragma("unroll") for (int e_ = 0; e_ < 2; ++e_) { const int n_ = ((g - 11) & 1) * 2 + e_; \
                kf[12 + n_] = (n_ < 3 && ptt == 0 && lane + n_ < 3) ? 0.f : bflo(ke[n_]); LAUNDER(kf[12 + n_]); } } \
        if (g >= 13 && g < 58) { const int t_ = ((g - 13) / 5) % 9, m_ = (g - 13) % 5, kb_ = t_ < 8 ? t_ : 12, sb_ = t_ < 8 ? 0 : 4; \
            if (m_ == 0) { px = rbf(kf[kb_] * sc[sb_ + 3]); py = rbf(kf[kb_ + 1] * sc[sb_ + 2]); } \
            if (m_ == 1) { px = rbf(px + py); py = kf[kb_ + 2] * sc[sb_ + 1]; } \
            if (m_ == 2) { px = rbf(px + rbf(py)); } \
            if (m_ == 3) { py = rbf(kf[kb_ + 3] * sc[sb_]); px += py; } \
            if (m_ == 4) { const unsigned w_ = t_ < 8 ? vr[(t_ >> 2) & 1][(t_ >> 1) & 1] : ke[4]; po[t_] = rbf(px) * ((t_ & 1) ? bfhi(w_) : bflo(w_)); LAUNDER(po[t_]); \
                           if (t_ & 1) { pw[3 - (t_ >> 1)] = pk2bf(po[t_], po[t_ - 1]); LAUNDER(pw[3 - (t_ >> 1)]); } } \
            LAUNDER(px); LAUNDER(py); } \
        if (g == 58) gstore16_sc1(pso, kvb, u32x4{pw[0], pw[1], pw[2], pw[3]}); \
        if (g == 59) asm volatile("global_store_short %0, %1, %2 sc1" :: "v"((63 - lane) * 2), "v"(pk2bf(po[8], 0.f)), "s"(kvb + (64 + wid) * (S_ * 2)) : "memory"); \
    } while (0)
    al[0] = G1_AH(rW, 0, 0); ah[0] = G1_AH(rW, 0, 1); xb[0][0] = G1_XF(0, 0);
    SBAR();
    xb[0][1] = G1_XF(0, 1); xb[0][2] = G1_XF(0, 2);
    SBAR();
    al[1] = G1_AH(rW, 1, 0); ah[1] = G1_AH(rW, 1, 1); xb[0][3] = G1_XF(0, 3);
    SBAR();
    char* inbox = ws + WS_INBOX + (size_t)bid * INBOX_B;
    const unsigned pmask = (bid == 0 ? 0x1554u : 0u) | (wid == 0 ? 0x1140u : 0u) | (wid < 6 ? 0x400u : 0u);
    int so_cur = 0;
    float gacc = 0.f, gt_, gu_; u32x2 gq, gg_;
    const unsigned gtW = lds0 + MI_GW + (lane & 15) * 8;
    const unsigned gtX = lds0 + G1_XOFF + ((mt2 & 1) * 4 + ks) * 4096 + (wid * 4 + (lane >> 4)) * 128 + ((((lane & 15) >> 1) ^ (((wid * 4 + (lane >> 4)) >> 1) & 7)) * 16) + (lane & 1) * 8;
#define GATE_OP(k) do { const int e_ = ((k) / 5) & 3, o_ = (k) % 5; \
        if (o_ == 0) { gt_ = (e_ & 1) ? bfhi(gq[e_ >> 1]) : bflo(gq[e_ >> 1]); LAUNDER(gt_); } \
        if (o_ == 1) { gu_ = (e_ & 1) ? bfhi(gg_[e_ >> 1]) : bflo(gg_[e_ >> 1]); LAUNDER(gu_); } \
        if (o_ == 2) { gt_ *= gu_; LAUNDER(gt_); } \
        if (o_ == 3) { gt_ = rbf(gt_); LAUNDER(gt_); } \
        if (o_ == 4) { gacc += gt_; LAUNDER(gacc); } } while (0)
#define G1_PROTO(u) do { \
    if ((u) == 10 && wid < 6) glds_m16(((vlin >> 4) & 15) * 128, (const char*)hk + (wid < 4 ? (size_t)ch0 * (S_ * 2) + wid * 2048 : (size_t)ch8 * (S_ * 2) + (wid & 1) * 2048), lds0 + MI_DUMMY);     \
    if (bid == 0 && (u) >= 2) {     \
        const unsigned pad_ = lds0 + (wid / 6) * G1_WSLOT + ((wid % 6) / 3) * G1_WMAT + (wid % 3) * G1_BLK + 2304; \
        if ((u) >= 4) gstore4_sc1_m(SY_ECHO + wid * 128 + (vlin >> 2), sync, ldsread4(pad_ + ((vlin >> 2) & 127)), 0xffffffffull); \
        if ((u) <= 10) asm volatile("s_mov_b32 m0, %2\n\ts_mov_b32 exec_hi, 0\n\tglobal_load_lds_dword %0, %1 sc1\n\ts_mov_b32 exec_hi, -1" :: "v"(vlin >> 2), "s"(sync + SY_NONCE + wid * 128), "s"(pad_) : "memory"); } \
    if (wid == 0) { \
        if ((u) == 6) { glds_poll(bid * 4, sync + SY_ECHO, lds0 + MI_POLL); } \
        else if ((u) == 8) { \
            if (__builtin_amdgcn_readfirstlane(ldsread4(lds0 + MI_POLL)) != nonce) { spin_wait(sync, SY_ECHO + bid * 4, nonce, 0u, bid, vlin); } \
            cnt_add(sync, CN_PREP + pdt); } \
        else if ((u) == 12) { glds_poll((vlin & 0x70) * 16, sync + SY_CNT + CN_PREP * 256, lds0 + MI_POLL); } \
    } } while (0)
#define G1_HOOK(u, i)
#define G1_PREBAR(u)
#define G1_SUB(u, P, WN, GL, NEXT, PEEL) do { \
        const int so_nxt = so_cur == 4 * G1_WSLOT ? 0 : so_cur + G1_WSLOT; \
        const int so_prv = so_cur == 0 ? 4 * G1_WSLOT : so_cur - G1_WSLOT; \
        const unsigned aC = rW + so_cur, aN = rW + so_nxt; \
        _Pragma("unroll") for (int i = 0; i < 18; ++i) { \
            if (i == 14 && (NEXT)) { G1_PREBAR(u); WAITV_BAR(WN); SBAR(); } \
            if ((PEEL) && P == 0) MFMA16Z(acc[i][0], CAT8(al[(i >> 1) % 3], ah[(i >> 1) % 3]), xb[P][2 * (i & 1)]); else MFMA16A(acc[i][0], CAT8(al[(i >> 1) % 3], ah[(i >> 1) % 3]), xb[P][2 * (i & 1)]); \
            SBAR(); \
            if ((i & 1) == 0) { if (i < 14) al[((i >> 1) + 2) % 3] = G1_AH(aC, (i >> 1) + 2, 0); else if (NEXT) al[((i >> 1) + 2) % 3] = G1_AH(aN, (i >> 1) - 7, 0); } \
            if (P == 0 && (i & 1) == 1 && i < 8) xb[1][i >> 1] = G1_XF((u) + 1, i >> 1); \
            if (P == 1 && (NEXT) && i == 15) xb[0][1] = G1_XF((u) + 1, 1); \
            if (PEEL) PREP_GAP(P * 36 + 2 * i); \
            if (P == 0 && i >= 4 && i < 14) GATE_OP(2 * (i - 4)); \
            SBAR(); \
            if ((PEEL) && P == 0) MFMA16Z(acc[i][1], CAT8(al[(i >> 1) % 3], ah[(i >> 1) % 3]), xb[P][2 * (i & 1) + 1]); else MFMA16A(acc[i][1], CAT8(al[(i >> 1) % 3], ah[(i >> 1) % 3]), xb[P][2 * (i & 1) + 1]); \
            SBAR(); \
            if ((i & 1) == 0) { if (i < 14) ah[((i >> 1) + 2) % 3] = G1_AH(aC, (i >> 1) + 2, 1); else if (NEXT) ah[((i >> 1) + 2) % 3] = G1_AH(aN, (i >> 1) - 7, 1); } \
            if (P == 1 && (NEXT) && i >= 14 && i <= 16) xb[0][i == 14 ? 0 : i - 13] = G1_XF((u) + 1, i == 14 ? 0 : i - 13); \
            if (PEEL) PREP_GAP(P * 36 + 2 * i + 1); \
            if (P == 0 && i >= 4 && i < 14) GATE_OP(2 * (i - 4) + 1); \
            SBAR(); \
            G1_HOOK(u, i); \
            if (P == 0 && i == 0) { gq = *(const LDSP u32x2*)(uintptr_t)(gtX + (((u) >> 1) & 1) * 32768); gg_ = *(const LDSP u32x2*)(uintptr_t)(gtW + ((u) >> 1) * 128); SBAR(); } \
            if (GL && P == 0 && !(PEEL) && i == 14) { if (__builtin_expect((pmask >> (u)) & 1, 0)) { G1_PROTO(u); } SBAR(); } \
            if ((PEEL) && P == 0 && i < 4) G1_X(1, i); \
            if ((PEEL) && P == 0 && i >= 4 && i < 10) G1_W(2 + (i - 4) / 3, (2 + (i - 4) / 3) * G1_WSLOT, (i - 4) % 3); \
            if (GL && i >= 14 && i <= 16) G1_W((u) + 4, so_prv, i - 14); \
            if (GL && P == 1 && i < 8 && (i & 1) == 0) G1_X(((u) >> 1) + 2, i >> 1); \
            SBAR(); \
        } \
        so_cur = so_nxt; \
    } while (0)
    G1_SUB(0, 0, 10, true, true, true);
    SBAR();
    G1_SUB(1, 1, 10, true, true, true);
    for (int u = 2; u < 14; u += 2) {
        G1_SUB(u, 0, LOOPN, true, true, false);
        G1_SUB(u + 1, 1, LOOPN, true, true, false);
    }
    LAUNDER(tid); lane = tid & 63;
    unsigned cvA, cvB, voffU[2], voff2[3], zaddr, baddr; int boff;
#define LATE_SETUP(c) do { \
        if ((c) == 0) { const int c_ = lane >> 4, wh_ = (lane >> 3) & 1, jx_ = lane & 7, ix_ = (wh_ ? 2040 : -8) + c_ + jx_; \
            baddr = lds0 + CV_BASE + (wid > 2 ? 2 : wid) * CV_CH + c_ * CV_HC + (wh_ ? 259 : 3) * 16 + jx_ * 2; LAUNDER(baddr); \
            boff = ix_ < 0 ? -2 : (ix_ > S_ - 1 ? -2 : ix_ * 2); LAUNDER(boff); } \
        if ((c) == 1 || (c) == 2) { int up_ = wid * 2 + (c) - 1; up_ = up_ > 14 ? 14 : up_; \
            const int ci_ = (up_ % 5) * 64 + lane, row_ = ci_ / 10, cc_ = ci_ - row_ * 10; \
            voffU[((c) - 1) & 1] = (up_ >= 10 ? ch8 : ch0 + up_ / 5) * (S_ * 2) + row_ * 128 + (cc_ < 8 ? cc_ : 0) * 16; LAUNDER(voffU[((c) - 1) & 1]); } \
        if ((c) == 3) { const int tz_ = tid < 3 * 162 ? tid : 0, ci_ = tz_ / 162, zf_ = tz_ - ci_ * 162; \
            zaddr = lds0 + CV_BASE + ci_ * CV_CH + (zf_ < 12 ? (zf_ / 3) * CV_HC + (zf_ % 3) * 16 : CV_U + 32 * 160 + (zf_ - 12) * 16); LAUNDER(zaddr); } \
        if ((c) == 4) { const int ap_ = lane & 15, g_ = lane >> 4; \
            cvA = ccb + ((ap_ + 1) & 3) * CV_HC + 2 * ((ap_ + 1) & ~3) + 16 * g_ + 32 * (wm & 1); cvB = ccb + CV_U + (15 - ap_) * 160 + 16 * g_; LAUNDER(cvA); LAUNDER(cvB); } \
        if ((c) >= 5) { const int c_ = ((c) - 5) * 48 + (lane < 48 ? lane : 0), r_ = c_ / 18; voff2[((c) - 5) % 3] = r_ * (D_ * 2) + (c_ - r_ * 18) * 16; LAUNDER(voff2[((c) - 5) % 3]); } \
        SBAR(); } while (0)
    unsigned bval;
    const char* hsrcA = (const char*)hk + (size_t)ch0 * (S_ * 2) + wm * 2 + wf * 2048; const char* hsrcB = (const char*)hk + (size_t)ch8 * (S_ * 2) + wm * 2 + wf * 2048;
    const unsigned hdst = lds0 + CV_BASE + wm * CV_HC + 64 + wf * 2048;
    const unsigned hmask = wf ? 0x7fffffffu : ~0u;
#undef G1_HOOK
#undef G1_PREBAR
#define CV_STAGE(z) do { \
        if ((z) < 6) asm volatile("s_add_u32 m0, %2, %3\n\ts_mov_b32 exec_hi, %4\n\tglobal_load_lds_dwordx4 %0, %1 offset:%5\n\ts_mov_b32 exec_hi, -1" \
                         :: "v"(vlin), "s"((z) >= 4 ? hsrcB : ((z) >= 2 ? hsrcA + S_ * 2 : hsrcA)), "s"(hdst), "i"(((z) >> 1) * CV_CH), "s"(((z) & 1) ? hmask : ~0u), "i"(((z) & 1) * 1024) : "memory", "scc"); \
        else { int up_ = wid * 2 + ((z) & 1); up_ = up_ > 14 ? 14 : up_; const int ci_ = up_ / 5; glds_sc1(voffU[(z) & 1], ws + WS_KVR, lds0 + CV_BASE + ci_ * CV_CH + CV_U + (up_ - ci_ * 5) * 1024); } \
        SBAR(); } while (0)
#define G1_HOOK(u, i) do { \
        if ((u) == 14 && i == 0) LATE_SETUP(0); \
        if (((u) == 15 && (i == 15 || i == 17)) || ((u) == 16 && (i == 1 || i == 3))) {     \
            int pc_ = ((u) == 15 ? (i - 15) >> 1 : 2 + (i >> 1)) * 8 + wid; pc_ = pc_ > 26 ? 26 : pc_; \
            gstore16_sc1(vlin, inbox + pc_ * 1024, u32x4{~0u, ~0u, ~0u, ~0u}); SBAR(); } \
        if ((u) == 14 && (i == 4 || i == 8 || i == 12)) {     \
            unsigned o_ = ((wid * 3 + (i >> 2) - 1) * 14 + ((lane & 15) < 14 ? (lane & 15) : 0)) * 128; o_ = o_ < 324 * 128 ? o_ : 0; \
            glds_m16(o_, (const char*)w2 + (size_t)(ks * 1152 + idx * 36) * (D_ * 2), lds0 + MI_DUMMY); SBAR(); } \
        if ((u) == 15 && i >= 4 && i < 18 && (i & 1) == 0) LATE_SETUP(1 + ((i - 4) >> 1)); \
        if ((u) == 14 && i == 2) gload2_sc1(bval, boff < 0 ? 0 : boff, (const char*)hk + (size_t)(wid >= 2 ? ch8 : ch0 + wid) * (S_ * 2)); \
        if ((u) == 16 && i == 15) CV_STAGE(0); if ((u) == 16 && i == 17) CV_STAGE(1); \
        if ((u) == 17 && i == 1) CV_STAGE(2); if ((u) == 17 && i == 3) CV_STAGE(3); if ((u) == 17 && i == 5) CV_STAGE(4); if ((u) == 17 && i == 7) CV_STAGE(5); \
        if ((u) == 17 && i == 9) CV_STAGE(6); if ((u) == 17 && i == 11) CV_STAGE(7); \
        if ((u) == 17 && i == 13) { asm volatile("" : "+v"(bval)); *(LDSP u32x4*)(uintptr_t)zaddr = u32x4{0, 0, 0, 0}; *(LDSP u16*)(uintptr_t)baddr = boff < 0 ? (u16)0 : (u16)bval; SBAR(); } \
    } while (0)
#define G1_PREBAR(u) do { if ((u) == 16 && wid == 0) {     \
            const unsigned sh_ = bid == 0 ? (vlin >> 4) & 7 : quad >> 3; \
            if (!wave_all(ldsread4(lds0 + MI_POLL + sh_ * 4) >= 32u)) { spin_wait(sync, SY_CNT + (CN_PREP + sh_) * 256, 32u, GE, bid, vlin); } } } while (0)
    G1_SUB(14, 0, 14, false, true, false);
    G1_SUB(15, 1, 7, false, true, false);
    G1_SUB(16, 0, 7, false, true, false);
    G1_SUB(17, 1, 0, false, false, false);
    asm volatile("s_nop 7" ::: "memory");
    SBAR();
    const char* w2src = (const char*)w2 + (size_t)(ks * 1152 + kh * 576 + wm * 16) * (D_ * 2) + nt * 288;
    const unsigned w2dst = lds0 + (kh * 8 + wm * 2) * G1_BLK;
#define G2_W(s, i) glds_48b(voff2[(i) % 3], w2src + (size_t)(s) * (64 * D_ * 2) + ((i) / 3) * (8 * D_ * 2), w2dst + ((s) & 3) * G2_STG + ((i) / 3) * G1_BLK + ((i) % 3) * 768)
    {
        gacc += asf(__builtin_amdgcn_update_dpp(0, __builtin_bit_cast(int, gacc), 0xB1, 0xf, 0xf, true));
        gacc += asf(__builtin_amdgcn_update_dpp(0, __builtin_bit_cast(int, gacc), 0x4E, 0xf, 0xf, true));
        gacc += asf(__builtin_amdgcn_update_dpp(0, __builtin_bit_cast(int, gacc), 0x141, 0xf, 0xf, true));
        gacc += asf(__builtin_amdgcn_update_dpp(0, __builtin_bit_cast(int, gacc), 0x140, 0xf, 0xf, true));
        *(LDSP float*)(uintptr_t)(lds0 + MI_G + (wid * 4 + (lane >> 4)) * 4) = gacc;
        char* hb = ws + WS_H + ((size_t)(mt1 * 16 + wm * 4) * 144 + ((9 * ft + 1) >> 1)) * 1024;
        const unsigned hso = (2 * ((lane >> 4) & 1) + (lane >> 5)) * (144 * 1024) + ((2 * fodd + wf) * 16 + (lane & 15)) * 16;
        float nl2e = -1.4426950408889634f; LAUNDER_S(nl2e);
        __builtin_amdgcn_s_setprio(2);
        SBAR();
        SFOR(j, 0, 9)
            f32x2 G[4], U[4], T[4]; unsigned pk[4];
            if constexpr (j < 8) {
#pragma unroll
                for (int e = 0; e < 4; ++e) {
                    constexpr int m = j & 3; const int a = 4 * (j >> 2) + 2 * (e >> 1);
                    G[e] = f32x2{acc[2 * a + (m >> 1)][m & 1][2 * (e & 1)], acc[2 * a + (m >> 1)][m & 1][2 * (e & 1) + 1]};
                    U[e] = f32x2{acc[2 * a + 2 + (m >> 1)][m & 1][2 * (e & 1)], acc[2 * a + 2 + (m >> 1)][m & 1][2 * (e & 1) + 1]};
                }
            } else {
#pragma unroll
                for (int e = 0; e < 8; ++e) {
                    auto sw = __builtin_amdgcn_permlane32_swap(asu(acc[16 + (e >> 2)][0][e & 3]), asu(acc[16 + (e >> 2)][1][e & 3]), false, false);
                    G[e >> 1][e & 1] = asf(sw[0]); U[e >> 1][e & 1] = asf(sw[1]);
                }
            }
#pragma unroll
            for (int e = 0; e < 4; ++e) T[e] = G[e] * nl2e;
#pragma unroll
            for (int e = 0; e < 4; ++e) { T[e][0] = __builtin_amdgcn_exp2f(T[e][0]); T[e][1] = __builtin_amdgcn_exp2f(T[e][1]); }
#pragma unroll
            for (int e = 0; e < 4; ++e) { G[e] = G[e] * U[e]; T[e] = T[e] + 1.f; }
#pragma unroll
            for (int e = 0; e < 4; ++e) { T[e][0] = __builtin_amdgcn_rcpf(T[e][0]); T[e][1] = __builtin_amdgcn_rcpf(T[e][1]); }
#pragma unroll
            for (int e = 0; e < 4; ++e) { G[e] = G[e] * T[e]; pk[e] = pk2bf(G[e][0], G[e][1]); }
            if constexpr (j < 8) gstore16_sc1(vlin, hb + (j & 3) * (144 * 1024) + (2 * wf + (j >> 2)) * 1024, u32x4{pk[0], pk[1], pk[2], pk[3]});
            else {
                auto s0 = __builtin_amdgcn_permlane16_swap(pk[0], pk[2], false, false);
                auto s1 = __builtin_amdgcn_permlane16_swap(pk[1], pk[3], false, false);
                gstore16_sc1(hso, hb + (fodd ? -1024 : 4096), u32x4{s0[0], s1[0], s0[1], s1[1]});
            }
            SBAR();
            if constexpr (j == 4) {
                asm volatile("s_waitcnt vmcnt(5)\n\ts_mov_b64 exec, 1\n\tds_add_u32 %0, %1 offset:4\n\ts_mov_b64 exec, -1" :: "v"(lds0 + MI_ARR), "v"(1u) : "memory");
                SBAR();
            }
        SEND
    }
    const char* hsrc0 = ws + WS_H + ((size_t)(mt2 * 8 + wm * 2 + kh) * 144 + ks * 36 + kh * 18) * 1024;
    const char* hsrc1 = ws + WS_H + ((size_t)(mt2 * 8 + wm * 2 + 1 - kh) * 144 + ks * 36 + kh * 18) * 1024;
    bf16x8 hf[5][4];
#define G2_H(s, i) gload16_sc1(hf[(s) % 5][i], vlin, (((i) & 1) ? hsrc1 : hsrc0) + ((s) * 2 + ((i) >> 1)) * 1024)
    unsigned hdone = 0; const unsigned vlin4 = vlin + 4096;
#define H_LD2(a, b, vo) "global_load_dwordx4 %" #a ", " vo ", %18 offset:" #b " sc1\n\t"
#define H_LD3(a, b, vo) "global_load_dwordx4 %" #a ", " vo ", %19 offset:" #b " sc1\n\t"
#define H_EARLY(go, C) asm volatile("s_cmp_eq_u32 %20, 0\n\ts_cbranch_scc1 1f\n\t" \
        H_LD2(0, 0, "%16") H_LD3(1, 0, "%16") H_LD2(2, 1024, "%16") H_LD3(3, 1024, "%16") H_LD2(4, 2048, "%16") H_LD3(5, 2048, "%16") H_LD2(6, 3072, "%16") H_LD3(7, 3072, "%16") \
        H_LD2(8, 0, "%17") H_LD3(9, 0, "%17") H_LD2(10, 1024, "%17") H_LD3(11, 1024, "%17") H_LD2(12, 2048, "%17") H_LD3(13, 2048, "%17") H_LD2(14, 3072, "%17") H_LD3(15, 3072, "%17") "1:" \
        : C(hf[0][0]), C(hf[0][1]), C(hf[0][2]), C(hf[0][3]), C(hf[1][0]), C(hf[1][1]), C(hf[1][2]), C(hf[1][3]), \
          C(hf[2][0]), C(hf[2][1]), C(hf[2][2]), C(hf[2][3]), C(hf[3][0]), C(hf[3][1]), C(hf[3][2]), C(hf[3][3]) \
        : "v"(vlin), "v"(vlin4), "s"(hsrc0), "s"(hsrc1), "s"(go) : "memory", "scc")
#define H_TRY(C) do { const unsigned go_ = (wave_all(ldsread4(lds0 + MI_ARR + 32 + (lane & 7) * 4) == 1u) ? 1u : 0u) & ~hdone; unsigned g2_ = __builtin_amdgcn_readfirstlane(go_); H_EARLY(g2_, C); hdone |= g2_; SBAR(); } while (0)
#define H_PUBLISH(N) do { asm volatile("s_waitcnt vmcnt(" #N ")" ::: "memory"); unsigned n_ = 0; \
        if (lane == 0) n_ = __hip_atomic_fetch_add((LDSP unsigned*)(uintptr_t)(lds0 + MI_ARR), 1u, __ATOMIC_RELAXED, __HIP_MEMORY_SCOPE_WORKGROUP); \
        if (__builtin_amdgcn_readfirstlane(n_) == 7u) gstore4_sc1_m(SY_HF + (mt1 * 32 + ft) * 128, sync, 1u, 1ull); } while (0)
    const float dD = (cch & 1) ? bfhi(sdw) : bflo(sdw);
    u16* yrow = (u16*)(ws + WS_Y2T) + (size_t)cch * S_;
    SBAR();
#define LDS_WAIT8(off) do { for (unsigned sp_ = 0; sp_ < (1u << 22) && __builtin_amdgcn_readfirstlane(ldsread4(lds0 + MI_ARR + (off))) < 8u; ++sp_) __builtin_amdgcn_s_sleep(1); asm volatile("" ::: "memory"); } while (0)
    {
        f32x4 c00 = {}, c01 = {}, c10 = {}, c11 = {};
        s16x4 El[8], Eh[8]; bf16x8 b1[8], b0[8];
#define CV_BO(F) (((F) >> 1) * 160 + ((F) & 1) * 64)
#define CV_E(F) CAT8(El[(F) & 7], Eh[(F) & 7])
#define CQ_LD(F) do { El[(F) & 7] = ldsread8v(cvA + 64 * (F)); Eh[(F) & 7] = ldsread8v(cvA + 64 * (F) + 8); b1[(F) & 7] = ldsread16(cvB + CV_BO(F)); } while (0)
#define LDS_FLAG(off, v) asm volatile("s_mov_b64 exec, 1\n\tds_add_u32 %0, %1\n\ts_mov_b64 exec, -1" :: "v"(lds0 + MI_ARR + (off)), "v"(v) : "memory")
        const int p = wm & 1;
        const unsigned red = lds0 + CV_RED + wm * 2048 + vlin;
        __builtin_amdgcn_s_setprio(0);
        LDS_WAIT8(4);
        if (wid < 4) {
#pragma unroll
            for (int F = 0; F < 3; ++F) { CQ_LD(F); b0[F] = ldsread16(cvB + 16 * 160 + CV_BO(F)); }
            SBAR();
#pragma unroll
            for (int F = 0; F < 32; ++F) {
                const int G = F + 3;
                if (F > 0) c01 = MFMA16(CV_E(F - 1), b1[F & 7], c01);
                if (G < 32) El[G & 7] = ldsread8v(cvA + 64 * G);
                SBAR();
                if (F > 0) c00 = MFMA16(CV_E(F - 1), b0[F & 7], c00);
                if (G < 32) Eh[G & 7] = ldsread8v(cvA + 64 * G + 8);
                SBAR();
                c11 = MFMA16(CV_E(F), b1[F & 7], c11);
                if (G < 32) b1[G & 7] = ldsread16(cvB + CV_BO(G));
                SBAR();
                c10 = MFMA16(CV_E(F), b0[F & 7], c10);
                if (G < 32) b0[G & 7] = ldsread16(cvB + 16 * 160 + CV_BO(G));
                SBAR();
                if (F == 1) { H_PUBLISH(0); SBAR(); }
                if (F >= D0 && F < D0 + 12) { G2_W((F - D0) / 6, (F - D0) % 6); SBAR(); }
            }
            *(LDSP f32x4*)(uintptr_t)red = c01; *(LDSP f32x4*)(uintptr_t)(red + 1024) = c11;
            LDS_FLAG(16 + wm * 4, 8u);
            c01 = f32x4{}; c11 = f32x4{};
            if (wm < 3) {
                const int dq = (2 - cci) * CV_CH;
                cvA += dq + 32 * ks - 64 - 32 * p + (wm == 1 ? 2048 : 0); cvB += dq + (wm >= 1 ? 16 * 160 : 0);
                CQ_LD(0); CQ_LD(1); CQ_LD(2); CQ_LD(3); CQ_LD(4);
                if (wm != 1 && ks < 2) { El[0] = s16x4{}; Eh[0] = s16x4{}; }
                SBAR();
#pragma unroll
                for (int F = 0; F < 32; ++F) {
                    if (F & 1) c01 = MFMA16(CV_E(F), b1[F & 7], c01); else c11 = MFMA16(CV_E(F), b1[F & 7], c11);
                    if (F + 5 < 32) CQ_LD(F + 5);
                    SBAR();
                }
                c01 += c11;
                const unsigned rq = lds0 + CV_RED + 8192 + vlin;
                if (wm == 1) { *(LDSP f32x4*)(uintptr_t)rq = c01; LDS_FLAG(8, 8u); }
                if (wm == 0) { LDS_WAIT8(8); c01 += *(const LDSP f32x4*)(uintptr_t)rq; }
            } else {
                spin_wait(sync, SY_HF + (mt1 * 32 + ks * 8 + (lane & 7)) * 128, 1u, 0u, 1u, vlin);
                *(LDSP unsigned*)(uintptr_t)(lds0 + MI_ARR + 32 + (lane & 7) * 4) = 1u;
            }
        } else {
            H_PUBLISH(0);
            El[7] = ldsread8v(cvA + 64 * 31); Eh[7] = ldsread8v(cvA + 64 * 31 + 8); CQ_LD(32); CQ_LD(33); CQ_LD(34); CQ_LD(35);
            SBAR();
#pragma unroll
            for (int F = 32; F < 64; ++F) {
                const int G = F + 4;
                c01 = MFMA16(CV_E(F - 1), b1[F & 7], c01);
                if (G < 64) { El[G & 7] = ldsread8v(cvA + 64 * G); Eh[G & 7] = ldsread8v(cvA + 64 * G + 8); }
                SBAR();
                c11 = MFMA16(CV_E(F), b1[F & 7], c11);
                if (G < 64) b1[G & 7] = ldsread16(cvB + CV_BO(G));
                SBAR();
                if (F >= D1 && F < D1 + 24 && ((F - D1) & 1) == 0) { G2_W((F - D1) / 12, ((F - D1) >> 1) % 6); SBAR(); }
            }
        }
        asm volatile("s_waitcnt vmcnt(6)" ::: "memory");
        H_TRY("=&v");
        if (wid >= 4) {
            LDS_WAIT8(16 + wm * 4);
            c00 = c01 + *(const LDSP f32x4*)(uintptr_t)red; c10 = c11 + *(const LDSP f32x4*)(uintptr_t)(red + 1024);
        }
        u32x2 u0 = conv_kv(ccb + CV_U, p, wf, lane), u1 = conv_kv(ccb + CV_U, p + 2, wf, lane), u2 = conv_kv(lds0 + CV_BASE + 2 * CV_CH + CV_U, ks, wid == 0, lane);
        SBAR();
        asm volatile("s_waitcnt lgkmcnt(0)\n\ts_barrier" ::: "memory");
        SBAR();
        H_EARLY(hdone ^ 1u, "+v");
        SBAR();
        conv_out(c00, u0, p, wf, lane, dD, yrow); conv_out(c10, u1, p + 2, wf, lane, dD, yrow);
        if (wid == 0 || wid == 2) conv_out(c01, u2, ks, wid == 0, lane, (ch8 & 1) ? bfhi(sdw8) : bflo(sdw8), (u16*)(ws + WS_Y2T) + (size_t)ch8 * S_);
        SBAR();
    }
    unsigned rW0 = lds0 + (lane >> 5) * G1_BLK + (4 * ((lane >> 4) & 1) + ((lane & 15) >> 2)) * 288 + (lane & 3) * 8;
    unsigned rWS = lds0 + 2 * G2_STG + (kh * 8 + (lane >> 4)) * G1_BLK + ((lane & 15) >> 2) * 288 + (lane & 3) * 8;
    unsigned rWs[2] = {rW0 + kh * 8 * G1_BLK, rW0 + kh * 8 * G1_BLK + 2 * G2_STG};
    LAUNDER(rW0); LAUNDER(rWS); LAUNDER(rWs[0]); LAUNDER(rWs[1]);
#define G2_KK(s, i) ((s) == 7 ? (i) & 1 : (i) / 9)
#define G2_A(s, i) ((s) == 7 ? (i) >> 1 : (i) % 9)
#define G2_SGL(s, i) ((2 * (s) + G2_KK(s, i)) % 9 == 4)
#define G2_AH(s, i, t) (G2_SGL(s, i) ? trread(rWS + G2_KK(s, i) * (4 * G1_BLK) + G2_A(s, i) * 32 + (t) * 4 * 288) \
                                     : trread(rWs[((s) >> 1) & 1] + ((s) & 1) * G2_STG + G2_KK(s, i) * (4 * G1_BLK) + G2_A(s, i) * 32 + (t) * 2 * G1_BLK))
#define G2_AH8(i, t) trread(rW0 + ((i) & 3) * (4 * G1_BLK) + ((i) >> 2) * 32 + (t) * 2 * G1_BLK)
#define G2_NXT(s, i, t) ((i) + 4 < 18 ? G2_AH(s, (i) + 4, t) : ((s) < 7 ? G2_AH((s) + 1, (i) + 4 - 18, t) : G2_AH8((i) + 4 - 18, t)))
#define G2_H8(i) gload16_sc1(hf[3][i], vlin, hsrc0 - kh * (18 * 1024) + (((i) >> 1) * 18 + 16 + ((i) & 1)) * 1024)
#define G2_WAIT(N, s) asm volatile("s_waitcnt vmcnt(" #N ")\n\ts_barrier" : "+v"(hf[(s) % 5][0]), "+v"(hf[(s) % 5][1]), "+v"(hf[(s) % 5][2]), "+v"(hf[(s) % 5][3]) :: "memory")
    f32x4 ac2[9][2];
    s16x4 bl[6], bh[6];
    unsigned yv[9], q8; u32x2 qf[2];
    const int fm = wid & 1, fpc = wid >> 1, row0 = mt2 * 128 + ks * 32;
    const int yt = row0 + 16 * fm;
    const char* yb = ws + WS_Y2T + (size_t)(nt * 144) * (S_ * 2) + ((yt >> 10) * 4 + ((yt >> 4) & 3)) * 512 + ((yt >> 6) & 15) * 32;
    const unsigned yo = (lane >> 4) * (S_ * 2) + (lane & 15) * 2;
    const unsigned xo = lds0 + FN_X + wid * 1024 + vlin, xi = lds0 + FN_X + (wid ^ 4) * 1024 + vlin;
    SBAR();
#pragma unroll
    for (int i = 0; i < 4; ++i) { bl[i] = G2_AH(0, i, 0); bh[i] = G2_AH(0, i, 1); }
    SBAR();
    asm volatile("s_waitcnt vmcnt(14)" : "+v"(hf[0][0]), "+v"(hf[0][1]), "+v"(hf[0][2]), "+v"(hf[0][3]) :: "memory");
    SBAR();
    SFOR(s, 0, 8)
        SFOR(i, 0, 18)
            if constexpr (i == 14 && s < 7) {
                if constexpr (s == 6) G2_WAIT(13, s + 1); else if constexpr (s == 0) G2_WAIT(16, s + 1); else if constexpr (s == 1) G2_WAIT(10, s + 1); else G2_WAIT(14, s + 1);
                SBAR();
                if (wid == 0 && (s == 1 || s == 3 || s == 5)) {
                    if (s == 1) cnt_add(sync, CN_CONV + nt);
                    if (s == 3) glds_poll(0, sync + SY_CNT + (CN_CONV + nt) * 256, lds0 + MI_POLL);
                    if (s == 5) { if (__builtin_amdgcn_readfirstlane(ldsread4(lds0 + MI_POLL)) < 64u) { spin_wait(sync, SY_CNT + (CN_CONV + nt) * 256, 64u, GE, 1u, vlin); } }
                    SBAR();
                }
            }
            if constexpr (i == 8 && s == 7) { G2_WAIT(9, 8); SBAR(); }
            {
                const bf16x8 A = CAT8(bl[i % 6], bh[i % 6]);
                if constexpr (s == 0 && i < 9) MFMA16Z(ac2[i][0], A, hf[0][0]); else MFMA16A(ac2[G2_A(s, i)][0], A, hf[s % 5][G2_KK(s, i) * 2]);
                SBAR();
                bl[(i + 4) % 6] = G2_NXT(s, i, 0);
                SBAR();
                if constexpr (s == 0 && i < 9) MFMA16Z(ac2[i][1], A, hf[0][1]); else MFMA16A(ac2[G2_A(s, i)][1], A, hf[s % 5][G2_KK(s, i) * 2 + 1]);
                SBAR();
                bh[(i + 4) % 6] = G2_NXT(s, i, 1);
            }
            if constexpr (s == 7 && (i & 1) == 1 && i >= 3) *(LDSP f32x4*)(uintptr_t)(xo + ((i >> 1) - 1) * 8192) = ac2[(i >> 1) - 1][1];
            SBAR();
            if constexpr (s == 0 && i < 12 && (i & 1) == 0) G2_W(2, i >> 1);
            if constexpr (i >= 14 && s + 3 < 9) G2_W(s + 3, i - 14);
            if constexpr (s >= 1 && i < 2 && s + 2 < 9) G2_W(s + 2, i + 4);
            if constexpr (s >= 1 && i >= 2 && i < 6 && s + 3 < 8) G2_H(s + 3, i - 2);
            if constexpr (s == 5 && i >= 2 && i < 6) G2_H8(i - 2);
            if constexpr ((s == 6 && i >= 14) || (s == 7 && i < 5)) {
                constexpr int e = s == 6 ? i - 14 : i + 4;
                if constexpr (e < 8) gload2_sc1(yv[e], yo * 4 - (lane & 15) * 6, yb + (size_t)(fpc * 32 + (e >> 2) * 16 + (e & 3)) * (S_ * 2));
                else gload2_sc1(yv[8], yo, yb + (size_t)(128 + 4 * fpc) * (S_ * 2));
            }
            if constexpr (s == 6 && (i == 7 || i == 9 || i == 11)) {
                const char* qb = (const char*)q + (size_t)(row0 + 16 * fm) * (D_ * 2) + nt * 288;
                if constexpr (i < 11) gload8_a(qf[(i - 7) >> 1], (lane & 15) * (D_ * 2) + (lane >> 4) * 8, qb + (2 * fpc + ((i - 7) >> 1)) * 32);
                else gload2_sc1(q8, (lane & 15) * (D_ * 2) + (lane >> 4) * 2, qb + 256 + fpc * 8);
            }
            SBAR();
        SEND
    SEND
    asm volatile("s_nop 7" : "+v"(ac2[8][1]));
    *(LDSP f32x4*)(uintptr_t)(xo + 8 * 8192) = ac2[8][1];
    SBAR();
    asm volatile("s_waitcnt lgkmcnt(0)\n\ts_barrier" ::: "memory");
    SBAR();
    float gg, g1, ga[9];
    {
        char* dst = ws + WS_INBOX + (size_t)((idx << 3) | (wm << 1) | (grp & 1)) * INBOX_B;
        const int src = wm == ks ? 0 : ks - (ks > wm ? 1 : 0);
        unsigned long long mown, msend;
        asm("s_cmp_eq_u32 %2, %3\n\ts_cselect_b64 %0, -1, 0\n\ts_cselect_b64 %1, 0, -1" : "=s"(mown), "=s"(msend) : "s"(wm), "s"(ks) : "scc");
        f32x4 c[2]; unsigned pk[4];
        SFOR(i, 0, 40)
            constexpr int a = i >> 2;
            if constexpr (i < 36) MFMA16A(ac2[a][0], CAT8(bl[i % 6], bh[i % 6]), hf[3][i & 3]);
            if constexpr (i == 36) asm volatile("s_nop 7" : "+v"(ac2[8][0]));
            SBAR();
            if constexpr (i + 4 < 36) { bl[(i + 4) % 6] = G2_AH8(i + 4, 0); bh[(i + 4) % 6] = G2_AH8(i + 4, 1); }
            if constexpr ((i & 3) == 0 && a < 9) c[a & 1] = *(const LDSP f32x4*)(uintptr_t)(xi + a * 8192);
            SBAR();
            if constexpr (i == 9) {
                asm volatile("s_waitcnt vmcnt(0)" : "+v"(yv[0]), "+v"(yv[1]), "+v"(yv[2]), "+v"(yv[3]), "+v"(yv[4]), "+v"(yv[5]), "+v"(yv[6]), "+v"(yv[7]), "+v"(yv[8]), "+v"(qf[0]), "+v"(qf[1]), "+v"(q8) :: "memory");
                const float sg = rbf(*(const LDSP float*)(uintptr_t)(lds0 + MI_G + (16 * fm + (lane & 15)) * 4));
                gg = rbf(__builtin_amdgcn_rcpf(1.f + __builtin_amdgcn_exp2f(sg * -1.4426950408889634f)));
                g1 = rbf(1.f - gg); LAUNDER(gg); LAUNDER(g1);
                SBAR();
            }
            if constexpr (i >= 11 && i < 29 && (i & 1) == 1) {
                constexpr int e = (i - 11) >> 1;
                const float qx = e == 8 ? bflo(q8) : ((e & 1) ? bfhi(qf[(e >> 2) & 1][(e >> 1) & 1]) : bflo(qf[(e >> 2) & 1][(e >> 1) & 1]));
                ga[e] = rbf(gg * rbf(qx * bflo(yv[e]))); LAUNDER(ga[e]);
                SBAR();
            }
            if constexpr ((i & 3) == 2 && a >= 1) {
                constexpr int b = a - 1;
                f32x4 sum = ac2[b][0] + c[b & 1];
                asm volatile("s_mov_b64 exec, %2\n\tds_write_b128 %0, %1 offset:%3\n\ts_mov_b64 exec, -1" :: "v"(b < 8 ? xi : xi + 8192), "v"(sum), "s"(mown), "i"((b < 8 ? b : 7) * 8192) : "memory");
                pk[2 * (b & 1)] = nosent(pk2bf(sum[0], sum[1])); pk[2 * (b & 1) + 1] = nosent(pk2bf(sum[2], sum[3]));
                if constexpr (b == 8) asm volatile("s_mov_b64 exec, %3\n\tglobal_store_dwordx2 %0, %1, %2 sc1\n\ts_mov_b64 exec, -1"
                                         :: "v"(lane * 8), "v"(u32x2{pk[0], pk[1]}), "s"(dst + 24576 + (kh * 3 + src) * 512), "s"(msend) : "memory");
                else if constexpr (b & 1) asm volatile("s_mov_b64 exec, %3\n\tglobal_store_dwordx4 %0, %1, %2 sc1\n\ts_mov_b64 exec, -1\n\ts_nop 0"
                                             :: "v"(vlin), "v"(u32x4{pk[0], pk[1], pk[2], pk[3]}), "s"(dst + ((kh * 4 + (b >> 1)) * 3 + src) * 1024), "s"(msend) : "memory");
                SBAR();
            }
        SEND
    }
    {
        const int tl = 16 * fm + (lane & 15), g4 = lane >> 4;
        const char* ib = inbox + (fm * 4 + fpc) * 3072;
        const char* ib8 = inbox + 24576 + fm * 1536;
        const unsigned o8 = (fpc * 16 + (lane & 15)) * 8 + (g4 >> 1) * 4;
        u32x4 in[3]; unsigned i8[3];
        unsigned first = 1;
#pragma nounroll
        for (unsigned spins = 0;; ++spins) {
            asm volatile("global_load_dwordx4 %0, %6, %7 sc1\n\tglobal_load_dwordx4 %1, %6, %7 offset:1024 sc1\n\tglobal_load_dwordx4 %2, %6, %7 offset:2048 sc1\n\t"
                         "global_load_dword %3, %8, %9 sc1\n\tglobal_load_dword %4, %8, %9 offset:512 sc1\n\tglobal_load_dword %5, %8, %9 offset:1024 sc1\n\t"
                         "s_cmp_eq_u32 %10, 0\n\ts_cbranch_scc1 1f\n\ts_waitcnt lgkmcnt(0)\n\ts_barrier\n1:\n\ts_waitcnt vmcnt(0)"
                         : "=&v"(in[0]), "=&v"(in[1]), "=&v"(in[2]), "=&v"(i8[0]), "=&v"(i8[1]), "=&v"(i8[2]) : "v"(vlin), "s"(ib), "v"(o8), "s"(ib8), "s"(first) : "memory", "scc");
            first = 0;
            unsigned mx = i8[0] > i8[1] ? i8[0] : i8[1]; mx = mx > i8[2] ? mx : i8[2];
#pragma unroll
            for (int j = 0; j < 3; ++j)
#pragma unroll
                for (int e = 0; e < 4; ++e) mx = mx > in[j][e] ? mx : in[j][e];
            if (wave_all(mx != 0xffffffffu) || spins >= SPIN_CAP) break;
        }
        float own[9];
        const unsigned pb = lds0 + FN_X + (((1 - fm) * 4 + ks) * 1024);
#pragma unroll
        for (int h = 0; h < 2; ++h) {
            const f32x4 p0 = *(const LDSP f32x4*)(uintptr_t)(pb + (2 * fpc + h) * 8192 + vlin);
#pragma unroll
            for (int e = 0; e < 4; ++e) own[4 * h + e] = p0[e];
        }
        own[8] = *(const LDSP float*)(uintptr_t)(pb + 8 * 8192 + (fpc * 16 + (lane & 15)) * 16 + g4 * 4);
        float o[9];
#pragma unroll
        for (int e = 0; e < 9; ++e) {
            float b = 0.f;
#pragma unroll
            for (int j = 0; j < 3; ++j) {
                if (e < 8) b += (e & 1) ? bfhi(in[j][e >> 1]) : bflo(in[j][e >> 1]);
                else b += (g4 & 1) ? bfhi(i8[j]) : bflo(i8[j]);
            }
            o[e] = ga[e] + rbf(g1 * rbf(b + own[e]));
        }
        u16* orow = out + (size_t)(row0 + tl) * D_ + nt * 144;
        *(u32x2*)(orow + fpc * 32 + g4 * 4) = u32x2{pk2bf(o[0], o[1]), pk2bf(o[2], o[3])};
        *(u32x2*)(orow + fpc * 32 + 16 + g4 * 4) = u32x2{pk2bf(o[4], o[5]), pk2bf(o[6], o[7])};
        orow[128 + fpc * 4 + g4] = (u16)pk2bf(o[8], 0.f);
    }
}

extern "C" void kernel_launch(void* const* d_in, const int* in_sizes, int n_in, void* d_out, int out_size, void* d_ws, size_t ws_size, hipStream_t stream) {
    (void)hipFuncSetAttribute((const void*)k_mega, hipFuncAttributeMaxDynamicSharedMemorySize, L_TOTAL);
    k_mega<<<256, 512, L_TOTAL, stream>>>((const u16*)d_in[0], (const u16*)d_in[1], (const u16*)d_in[2], (const u16*)d_in[3], (const u16*)d_in[4], (const u16*)d_in[5],
                                          (const u16*)d_in[7], (const u16*)d_in[8], (const u16*)d_in[9], (const u16*)d_in[10], (u16*)d_out, (char*)d_ws);
}
```
